```python
import torch

def _softmax(x, axis=-1):
    return torch.softmax(x, dim=axis)
def _gelu(x):
    return torch.nn.functional.gelu(x, approximate="tanh")
def _silu(x):
    return torch.nn.functional.silu(x)
def _sigmoid(x):
    return torch.sigmoid(x)
def _layernorm(x, axis=-1, eps=1e-5):
    m = x.mean(dim=axis, keepdim=True)
    v = x.var(dim=axis, keepdim=True, unbiased=False)
    return (x - m) / torch.sqrt(v + eps)
def _scatter_add(tgt, idx, src, axis=0):
    return tgt.clone().index_add_(axis, idx, src)
def _segment_sum(src, seg, n):
    out = torch.zeros((n,) + src.shape[1:], dtype=src.dtype)
    return out.index_add_(0, seg, src)
def _pad(x, pw):
    flat = tuple(v for pair in reversed(pw) for v in pair)
    return torch.nn.functional.pad(x, flat)

kernel_name = 'standalone__gqa_softmax__near_ridge'

def setup_inputs():

    _w = (lambda a: (lambda t: t.to(torch.bfloat16) if t.is_floating_point() else t)(torch.as_tensor(a)))
    B, Hq, Hkv, Sq, Sk, D = 8, 32, 8, 48, 2048, 128
    return {
        "q": _w((lambda *s: torch.randn(*s, dtype=torch.bfloat16) * 0.8)(B, Hq, Sq, D)),
        "k": _w((lambda *s: torch.randn(*s, dtype=torch.bfloat16) * 0.8)(B, Hkv, Sk, D)),
        "v": _w((lambda *s: torch.randn(*s, dtype=torch.bfloat16) * 0.05)(B, Hkv, Sk, D)),
    }

def reference(q, k, v):
    B, Hq, Sq, D = q.shape
    Hkv = k.shape[1]
    rep = Hq // Hkv
    k = (lambda x, n, axis: torch.repeat_interleave(x, n, dim=axis))(k, rep, axis=1)
    v = (lambda x, n, axis: torch.repeat_interleave(x, n, dim=axis))(v, rep, axis=1)
    scale = D ** -0.5
    scores = torch.einsum("bhqd,bhkd->bhqk", q, k) * scale
    p = _softmax(scores, axis=-1)
    return torch.einsum("bhqk,bhkd->bhqd", p, v)
```

```cpp
#include <hip/hip_runtime.h>
#include <hip/hip_bf16.h>
#include <cstdio>

#define ATTN_B 8
#define ATTN_HQ 32
#define ATTN_HKV 8
#define ATTN_SQ 48
#define ATTN_SK 2048
#define ATTN_NSPLIT 4
#define ATTN_THR 8
#define ATTN_KVCONV 0
#define ATTN_CAUSAL 0
#define ATTN_LENS 0
#define ATTN_QPARK (4 + ATTN_LENS)
#define ATTN_WG4 1
#define ATTN_QPARK4 4
#define ATTN_XCHG4 1
constexpr int B = ATTN_B, HQ = ATTN_HQ, HKV = ATTN_HKV, SQ = ATTN_SQ, SK = ATTN_SK;
constexpr int NSPLIT = ATTN_NSPLIT;
constexpr float THR = ATTN_THR;
constexpr int CAUSAL = ATTN_CAUSAL;
constexpr int HAS_LENS = ATTN_LENS;
constexpr int KV_CONVERT = ATTN_KVCONV;
constexpr int QPARK = ATTN_QPARK;
constexpr int WG4 = ATTN_WG4;
constexpr int XCHG4 = ATTN_XCHG4;
constexpr int QPARK4 = ATTN_QPARK4;
constexpr int D = 128, NW = 8, QBLK = 32, KVBLK = 64;
constexpr float SCALE = 0.088388347648318447f;
constexpr int G = HQ / HKV, ROWS = G * SQ, NT = SK / KVBLK, TPS = NT / NSPLIT;
constexpr int NWB = (WG4 && ROWS <= QBLK * 4 && !CAUSAL && !HAS_LENS && TPS % 4 == 0) ? 4 : NW, NSB = NSPLIT * (NW / NWB), TPSB = NT / NSB;
static_assert(HQ % HKV == 0 && ROWS >= 1 && ROWS <= QBLK * NW, "G*SQ must fit the workgroup's 256 MFMA rows (larger SQ -> dense body)");
static_assert(!(CAUSAL && HAS_LENS && !KV_CONVERT), "CAUSAL + HAS_LENS on the f32 convert-on-load loop spills 2 VGPRs (ROCm 7.2, QPARK 4..8): set KV_CONVERT 1 (bf16 loop, 0 spill) for that combination");
static_assert(SK % KVBLK == 0 && NT % NSPLIT == 0 && TPS >= 2 && TPS % 2 == 0, "SK must give every split an even number (>= 2) of 64-key tiles");
static_assert(NT % NSB == 0 && TPSB >= 2 && TPSB % 2 == 0, "internal: half-split tile count (NWB falls back to 8 waves when TPS % 4 != 0)");
constexpr float L2E = 1.4426950408889634f, SL2E = SCALE * L2E, THR2 = THR * L2E;
constexpr size_t SHM_V = KVBLK * D * 2, SHM_K = KVBLK * D * 2, SHM_WS = 2 * SHM_V + 2 * SHM_K;
template <int NWT> constexpr size_t shm_qp() { return SHM_WS + ((NWT == NW || !XCHG4) ? (size_t)NWT * 64 * 4 : 0); }
static_assert(QPARK >= 0 && QPARK <= 8 && QPARK4 >= 0 && QPARK4 <= 8, "QPARK");
constexpr size_t WS_PML = 4096, PML_BYTES = (size_t)B * HQ * SQ * NSB * 2 * 4;
constexpr size_t WS_PO = (WS_PML + PML_BYTES + 255) / 256 * 256, WS_PEND = NSB > 1 ? WS_PO + (size_t)B * HQ * SQ * NSB * D * 4 : WS_PML;
constexpr size_t NQ = (size_t)B * HQ * SQ * D, NKV = (size_t)B * HKV * SK * D;
constexpr size_t WS_QB = (WS_PEND + 255) / 256 * 256, WS_KB = WS_QB + NQ * 2, WS_VB = WS_KB + NKV * 2;
constexpr size_t WS_END = KV_CONVERT ? WS_VB + NKV * 2 : WS_PEND;

using bf16 = __hip_bfloat16;
using bf16x8 = __attribute__((ext_vector_type(8))) short;
using s16x4 = __attribute__((ext_vector_type(4))) short;
using f32x16 = __attribute__((ext_vector_type(16))) float;
using f32x4 = __attribute__((ext_vector_type(4))) float;
using u32x4 = __attribute__((ext_vector_type(4))) unsigned;
template <class T> struct InCfg;
template <> struct InCfg<__hip_bfloat16> { static constexpr int NWT = NWB, QP = NWB == 4 ? QPARK4 : 0; };
template <> struct InCfg<float> { static constexpr int NWT = NW, QP = QPARK; };
template <class T> constexpr int nw_for() { return InCfg<T>::NWT; }
template <class T> constexpr int ns_for() { return NSPLIT * (NW / InCfg<T>::NWT); }
template <class T> constexpr size_t shm_bytes() { return shm_qp<InCfg<T>::NWT>() + (size_t)InCfg<T>::QP * 64 * InCfg<T>::NWT * 16; }
#define KSWZ(row, colB) ((row) * 256 + ((colB) ^ (((row) & 7) << 4)))
#define SBAR() __builtin_amdgcn_sched_barrier(0)
__device__ __forceinline__ int crow(int r, int hi) { return (r & 3) + 8 * (r >> 2) + 4 * hi; }
__device__ __forceinline__ unsigned cvtpk(float lo, float hi) { unsigned r; asm volatile("v_cvt_pk_bf16_f32 %0, %1, %2" : "=v"(r) : "v"(lo), "v"(hi)); return r; }
__device__ __forceinline__ bf16x8 pack8(f32x4 a, f32x4 b) { u32x4 w = {cvtpk(a[0], a[1]), cvtpk(a[2], a[3]), cvtpk(b[0], b[1]), cvtpk(b[2], b[3])}; return *reinterpret_cast<bf16x8*>(&w); }
__device__ __forceinline__ bf16x8 ld8(const bf16* p) { return *reinterpret_cast<const bf16x8*>(p); }
__device__ __forceinline__ bf16x8 ld8(const float* p) { return pack8(*reinterpret_cast<const f32x4*>(p), *reinterpret_cast<const f32x4*>(p + 4)); }
template <class T, int NR> struct Stage;
template <> struct Stage<bf16, 2> { bf16x8 v0, v1, k0, k1;
  __device__ __forceinline__ void load(const bf16* V0, const bf16* V1, const bf16* K0, const bf16* K1) { v0 = ld8(V0); v1 = ld8(V1); k0 = ld8(K0); k1 = ld8(K1); }
  __device__ __forceinline__ bf16x8 gv0() const { return v0; } __device__ __forceinline__ bf16x8 gv1() const { return v1; }
  __device__ __forceinline__ bf16x8 gk0() const { return k0; } __device__ __forceinline__ bf16x8 gk1() const { return k1; } };
template <> struct Stage<float, 2> { f32x4 v0a, v0b, v1a, v1b, k0a, k0b, k1a, k1b;
  __device__ __forceinline__ void load(const float* V0, const float* V1, const float* K0, const float* K1) {
    v0a = *(const f32x4*)V0; v0b = *(const f32x4*)(V0 + 4); v1a = *(const f32x4*)V1; v1b = *(const f32x4*)(V1 + 4);
    k0a = *(const f32x4*)K0; k0b = *(const f32x4*)(K0 + 4); k1a = *(const f32x4*)K1; k1b = *(const f32x4*)(K1 + 4); }
  __device__ __forceinline__ bf16x8 gv0() const { return pack8(v0a, v0b); } __device__ __forceinline__ bf16x8 gv1() const { return pack8(v1a, v1b); }
  __device__ __forceinline__ bf16x8 gk0() const { return pack8(k0a, k0b); } __device__ __forceinline__ bf16x8 gk1() const { return pack8(k1a, k1b); } };
template <int NR> struct Stage<bf16, NR> { bf16x8 v[NR], k[NR];
  __device__ __forceinline__ void ldv(int i, const bf16* p) { v[i] = ld8(p); } __device__ __forceinline__ void ldk(int i, const bf16* p) { k[i] = ld8(p); }
  __device__ __forceinline__ bf16x8 gv(int i) const { return v[i]; } __device__ __forceinline__ bf16x8 gk(int i) const { return k[i]; } };
template <int NR> struct Stage<float, NR> { f32x4 va[NR], vb[NR], ka[NR], kb[NR];
  __device__ __forceinline__ void ldv(int i, const float* p) { va[i] = *(const f32x4*)p; vb[i] = *(const f32x4*)(p + 4); }
  __device__ __forceinline__ void ldk(int i, const float* p) { ka[i] = *(const f32x4*)p; kb[i] = *(const f32x4*)(p + 4); }
  __device__ __forceinline__ bf16x8 gv(int i) const { return pack8(va[i], vb[i]); } __device__ __forceinline__ bf16x8 gk(int i) const { return pack8(ka[i], kb[i]); } };
__device__ __forceinline__ int voff(int row, int sc) { return (row >> 5) * 8192 + 2 * ((sc >> 4) * 512 + (row & 31) * 16 + (sc & 8)); }

__device__ __forceinline__ void partialSM(f32x16& p0, f32x16& p1, float& m_reg, float& msub, float& alpha, int kbase, int lim, int lim0) {
  if constexpr (CAUSAL || HAS_LENS) { if (kbase + KVBLK - 1 > lim0) {
    const float ninf = -__builtin_inff(); const int hi = (threadIdx.x & 63) >> 5;
    for (int r = 0; r < 16; ++r) { const int k = kbase + crow(r, hi); p0[r] = k > lim ? ninf : p0[r]; p1[r] = k + 32 > lim ? ninf : p1[r]; } } }
  float pmax = p0[0]; for (int r = 1; r < 16; ++r) pmax = fmaxf(pmax, p0[r]); for (int r = 0; r < 16; ++r) pmax = fmaxf(pmax, p1[r]);
  { auto rr = __builtin_amdgcn_permlane32_swap(__float_as_uint(pmax), __float_as_uint(pmax), false, false);
    pmax = fmaxf(__uint_as_float(rr[0]), __uint_as_float(rr[1])); }
  pmax *= SL2E;
  if (__builtin_expect(__all(pmax - m_reg <= THR2), 1)) { alpha = 1.f; }
  else { float mn = fmaxf(m_reg, pmax); msub = (mn == -__builtin_inff()) ? 0.f : mn; alpha = __builtin_amdgcn_exp2f(m_reg - msub); m_reg = mn; }
  float nm = -msub;
  for (int r = 0; r < 16; ++r) p0[r] = fmaf(p0[r], SL2E, nm); for (int r = 0; r < 16; ++r) p1[r] = fmaf(p1[r], SL2E, nm);
  for (int r = 0; r < 16; ++r) p0[r] = __builtin_amdgcn_exp2f(p0[r]);
}
__device__ __forceinline__ void finishSM(f32x16& p0, f32x16& p1, float alpha, float& l_reg, bf16x8& pa0, bf16x8& pa1, bf16x8& pa2, bf16x8& pa3) {
  for (int r = 0; r < 16; ++r) p1[r] = __builtin_amdgcn_exp2f(p1[r]);
  float ps = 0; for (int r = 0; r < 16; ++r) ps += p0[r]; for (int r = 0; r < 16; ++r) ps += p1[r];
  { auto rr = __builtin_amdgcn_permlane32_swap(__float_as_uint(ps), __float_as_uint(ps), false, false); ps = __uint_as_float(rr[0]) + __uint_as_float(rr[1]); }
  l_reg = l_reg * alpha + ps;
#define PK4(P, BASE, OUT) do { unsigned a0 = cvtpk(P[BASE + 0], P[BASE + 1]), a1 = cvtpk(P[BASE + 2], P[BASE + 3]); \
    unsigned b0 = cvtpk(P[BASE + 4], P[BASE + 5]), b1 = cvtpk(P[BASE + 6], P[BASE + 7]); \
    auto r0 = __builtin_amdgcn_permlane32_swap(a0, b0, false, false); auto r1 = __builtin_amdgcn_permlane32_swap(a1, b1, false, false); \
    u32x4 w = {r0[0], r1[0], r0[1], r1[1]}; OUT = *reinterpret_cast<bf16x8*>(&w); } while (0)
  PK4(p0, 0, pa0); PK4(p0, 8, pa1); PK4(p1, 0, pa2); PK4(p1, 8, pa3);
#undef PK4
}
template <int QP, int NWT> __device__ __forceinline__ void qkt(f32x16& p0, f32x16& p1, const bf16* Ks, const bf16x8* qr, const char* qp, int r32, int hi) {
  p0 = f32x16{}; p1 = f32x16{};
#pragma unroll
  for (int d0 = 0; d0 < 8; ++d0) { int cb = (d0 * 16 + hi * 8) * 2;
    bf16x8 b0 = *reinterpret_cast<const bf16x8*>((const char*)Ks + KSWZ(r32, cb));
    bf16x8 b1 = *reinterpret_cast<const bf16x8*>((const char*)Ks + KSWZ(32 + r32, cb));
    const bf16x8 q = d0 < 8 - QP ? qr[d0] : *reinterpret_cast<const bf16x8*>(qp + (d0 - (8 - QP)) * (64 * NWT * 16));
    p0 = __builtin_amdgcn_mfma_f32_32x32x16_bf16(b0, q, p0, 0, 0, 0);
    p1 = __builtin_amdgcn_mfma_f32_32x32x16_bf16(b1, q, p1, 0, 0, 0); }
}
__device__ __forceinline__ void pv_d0(f32x16* o, int vb, bf16x8 pa0, bf16x8 pa1, bf16x8 pa2, bf16x8 pa3) {
#pragma unroll
  for (int d0 = 0; d0 < 4; ++d0) { s16x4 lo[4], hi[4];
#pragma unroll
    for (int ks = 0; ks < 4; ++ks) { int a = vb + d0 * 2048 + (ks >> 1) * 8192 + (ks & 1) * 512;
      asm volatile("ds_read_b64_tr_b16 %0,%1" : "=&v"(lo[ks]) : "v"(a) : "memory");
      asm volatile("ds_read_b64_tr_b16 %0,%1 offset:128" : "=&v"(hi[ks]) : "v"(a) : "memory"); }
    asm volatile("s_waitcnt lgkmcnt(0)" ::: "memory"); SBAR();
#define PK(k) (bf16x8){lo[k][0], lo[k][1], lo[k][2], lo[k][3], hi[k][0], hi[k][1], hi[k][2], hi[k][3]}
    o[d0] = __builtin_amdgcn_mfma_f32_32x32x16_bf16(pa0, PK(0), o[d0], 0, 0, 0);
    o[d0] = __builtin_amdgcn_mfma_f32_32x32x16_bf16(pa1, PK(1), o[d0], 0, 0, 0);
    o[d0] = __builtin_amdgcn_mfma_f32_32x32x16_bf16(pa2, PK(2), o[d0], 0, 0, 0);
    o[d0] = __builtin_amdgcn_mfma_f32_32x32x16_bf16(pa3, PK(3), o[d0], 0, 0, 0);
#undef PK
  }
}

template <class TQ, class TKV>
__global__ __launch_bounds__(64 * InCfg<TKV>::NWT, NW / InCfg<TKV>::NWT)
void gqa_chunk_fwd(const TQ* __restrict__ Q, const TKV* __restrict__ K, const TKV* __restrict__ V, const int* __restrict__ lens, float* __restrict__ O, float* __restrict__ pml, float* __restrict__ po) {
  constexpr int NWT = InCfg<TKV>::NWT, NS = NSPLIT * (NW / NWT), TPS = NT / NS;
  constexpr int NR = KVBLK / (4 * NWT);
  static_assert(ROWS <= QBLK * NWT, "rows must fit this form's waves");
  const int tid = threadIdx.x, wid = tid >> 6, lane = tid & 63, r32 = lane & 31, hi = lane >> 5;
  const int split = blockIdx.x, kvh = blockIdx.y, b = blockIdx.z;
  const TQ* Qg = Q + ((size_t)b * HQ + (size_t)kvh * G) * SQ * D;
  const TKV* Kh = K + (((size_t)b * HKV + kvh) * SK + (size_t)split * TPS * KVBLK) * D;
  const TKV* Vh = V + (((size_t)b * HKV + kvh) * SK + (size_t)split * TPS * KVBLK) * D;
  extern __shared__ __attribute__((aligned(16))) char shm[];
  bf16* V_lds = (bf16*)shm; bf16* K_lds = (bf16*)(shm + 2 * SHM_V);
  float* ws = (float*)(shm + SHM_WS) + wid * 64; float* li_l = ws; float* al_l = ws + 32;
  constexpr int QP = InCfg<TKV>::QP; char* qp = shm + shm_qp<NWT>() + tid * 16;
  const int R = wid * QBLK + r32, Rc = R < ROWS ? R : ROWS - 1;
  int len = SK; if constexpr (HAS_LENS) { len = __builtin_amdgcn_readfirstlane(lens[b]); len = len < 0 ? 0 : (len > SK ? SK : len); }
  const int lim0 = CAUSAL ? len - SQ : len - 1, lim = CAUSAL ? lim0 + Rc % SQ : lim0, kb0 = split * TPS * KVBLK;
  bf16x8 qr[8]; const TQ* qrow = Qg + (size_t)Rc * D;
  float m_reg = -__builtin_inff(), msub = 0.f, l_reg = 0.f; f32x16 o[4] = {};
  const int sr = tid >> 4, sc = (tid & 15) * 8;
  const int vb0 = (int)(uintptr_t)V_lds + (lane & 15) * 8 + ((lane >> 4) & 1) * 1024 + (lane >> 5) * 256;
  Stage<TKV, NR> st; int sk0 = 0; (void)sk0;
#define SLOAD(k0) do { sk0 = kb0 + (k0); \
    if constexpr (NR == 2) st.load(Vh + (size_t)((k0) + sr) * D + sc, Vh + (size_t)((k0) + 32 + sr) * D + sc, Kh + (size_t)((k0) + sr) * D + sc, Kh + (size_t)((k0) + 32 + sr) * D + sc); \
    else { const TKV* vr_ = Vh + (size_t)((k0) + NR * sr) * D + sc; const TKV* kr_ = Kh + (size_t)((k0) + NR * sr) * D + sc; \
           for (int i_ = 0; i_ < NR; ++i_) st.ldv(i_, vr_ + i_ * D); for (int i_ = 0; i_ < NR; ++i_) st.ldk(i_, kr_ + i_ * D); } } while (0)
#define SWRITE(buf) do { if constexpr (NR == 2) { bf16x8 v0_ = st.gv0(), v1_ = st.gv1(), k0_ = st.gk0(), k1_ = st.gk1(); \
      if constexpr (HAS_LENS) { if (sk0 + KVBLK > len) { const bf16x8 z_ = {}; if (sk0 + sr >= len) { v0_ = z_; k0_ = z_; } if (sk0 + 32 + sr >= len) { v1_ = z_; k1_ = z_; } } } \
      *(bf16x8*)((char*)V_lds + (buf) * SHM_V + voff(sr, sc)) = v0_; *(bf16x8*)((char*)V_lds + (buf) * SHM_V + voff(32 + sr, sc)) = v1_; int kc = sc * 2; \
      *(bf16x8*)((char*)K_lds + (buf) * SHM_K + KSWZ(sr, kc)) = k0_; *(bf16x8*)((char*)K_lds + (buf) * SHM_K + KSWZ(32 + sr, kc)) = k1_; } \
    else { for (int i_ = 0; i_ < NR; ++i_) { const int row_ = NR * sr + i_; bf16x8 v_ = st.gv(i_), k_ = st.gk(i_); \
      if constexpr (HAS_LENS) { if (sk0 + KVBLK > len) { if (sk0 + row_ >= len) { v_ = bf16x8{}; k_ = bf16x8{}; } } } \
      *(bf16x8*)((char*)V_lds + (buf) * SHM_V + voff(row_, sc)) = v_; *(bf16x8*)((char*)K_lds + (buf) * SHM_K + KSWZ(row_, sc * 2)) = k_; } } } while (0)
#define RESC(a) do { if (__any((a) < 1.f)) { if constexpr (NWT == NW || !XCHG4) { if (hi == 0) al_l[r32] = (a); asm volatile("s_waitcnt lgkmcnt(0)" ::: "memory"); \
      for (int d = 0; d < 4; ++d) for (int r = 0; r < 16; ++r) o[d][r] *= al_l[crow(r, hi)]; } \
    else { const int ai_ = __float_as_int(a);    \
      for (int r = 0; r < 16; ++r) { const float a0_ = __int_as_float(__builtin_amdgcn_readlane(ai_, crow(r, 0))), a1_ = __int_as_float(__builtin_amdgcn_readlane(ai_, crow(r, 1))); \
        const float ar_ = hi ? a1_ : a0_; for (int d = 0; d < 4; ++d) o[d][r] *= ar_; } } } } while (0)
  f32x16 pA0, pA1, pB0, pB1; float alA, alB; bf16x8 pa0, pa1, pa2, pa3;
  for (int d0 = 0; d0 < 8; ++d0) qr[d0] = ld8(qrow + d0 * 16 + hi * 8);
  for (int j = 0; j < QP; ++j) *reinterpret_cast<bf16x8*>(qp + j * (64 * NWT * 16)) = qr[8 - QP + j];
  SLOAD(0); SWRITE(0); __syncthreads();
  qkt<QP, NWT>(pA0, pA1, K_lds, qr, qp, r32, hi); partialSM(pA0, pA1, m_reg, msub, alA, kb0, lim, lim0);
  SLOAD(KVBLK); SWRITE(1); __syncthreads();
  for (int j = 1; j + 1 < TPS; j += 2) {
    SBAR(); qkt<QP, NWT>(pB0, pB1, (bf16*)((char*)K_lds + SHM_K), qr, qp, r32, hi);
    finishSM(pA0, pA1, alA, l_reg, pa0, pa1, pa2, pa3); SBAR();
    SLOAD((j + 1) * KVBLK); SBAR();
    pv_d0(o, vb0, pa0, pa1, pa2, pa3); partialSM(pB0, pB1, m_reg, msub, alB, kb0 + j * KVBLK, lim, lim0);
    __syncthreads(); asm volatile("s_waitcnt vmcnt(0)" ::: "memory"); SWRITE(0);
    RESC(alB); __syncthreads();
    SBAR(); qkt<QP, NWT>(pA0, pA1, K_lds, qr, qp, r32, hi);
    finishSM(pB0, pB1, alB, l_reg, pa0, pa1, pa2, pa3); SBAR();
    if (j + 2 < TPS) { SLOAD((j + 2) * KVBLK); SBAR(); }
    pv_d0(o, vb0 + (int)SHM_V, pa0, pa1, pa2, pa3); partialSM(pA0, pA1, m_reg, msub, alA, kb0 + (j + 1) * KVBLK, lim, lim0);
    __syncthreads(); if (j + 2 < TPS) { asm volatile("s_waitcnt vmcnt(0)" ::: "memory"); SWRITE(1); }
    RESC(alA); __syncthreads();
  }
  SBAR(); qkt<QP, NWT>(pB0, pB1, (bf16*)((char*)K_lds + SHM_K), qr, qp, r32, hi);
  finishSM(pA0, pA1, alA, l_reg, pa0, pa1, pa2, pa3); SBAR();
  pv_d0(o, vb0, pa0, pa1, pa2, pa3); partialSM(pB0, pB1, m_reg, msub, alB, kb0 + (TPS - 1) * KVBLK, lim, lim0);
  __syncthreads(); RESC(alB);
  finishSM(pB0, pB1, alB, l_reg, pa0, pa1, pa2, pa3); SBAR();
  pv_d0(o, vb0 + (int)SHM_V, pa0, pa1, pa2, pa3);
#undef SLOAD
#undef SWRITE
#undef RESC
  if constexpr (NS == 1) {
    float rli[16];
    if constexpr (NWT == NW || !XCHG4) { if (hi == 0) li_l[r32] = l_reg; asm volatile("s_waitcnt lgkmcnt(0)" ::: "memory");
#pragma unroll
      for (int r = 0; r < 16; ++r) { float l = li_l[crow(r, hi)]; rli[r] = l > 0.f ? __builtin_amdgcn_rcpf(l) : 0.f; } }
    else {
#pragma unroll
      for (int r = 0; r < 16; ++r) { const int li_ = __float_as_int(l_reg); const float l0 = __int_as_float(__builtin_amdgcn_readlane(li_, crow(r, 0))), l1 = __int_as_float(__builtin_amdgcn_readlane(li_, crow(r, 1)));
        const float l = hi ? l1 : l0; rli[r] = l > 0.f ? __builtin_amdgcn_rcpf(l) : 0.f; } }
#pragma unroll
    for (int r = 0; r < 16; ++r) { const int Ro = wid * QBLK + crow(r, hi); if (Ro < ROWS) { float* orow = O + (((size_t)b * HQ + (size_t)kvh * G) * SQ + Ro) * D;
        for (int d0 = 0; d0 < 4; ++d0) orow[d0 * 32 + r32] = o[d0][r] * rli[r]; } }
  } else {
    const size_t row0 = ((size_t)b * HQ + (size_t)kvh * G) * SQ;
    if (hi == 0 && R < ROWS) { float* p = pml + ((row0 + R) * NS + split) * 2; p[0] = m_reg; p[1] = l_reg; }
#pragma unroll
    for (int r = 0; r < 16; ++r) { const int Ro = wid * QBLK + crow(r, hi); if (Ro < ROWS) { float* prow = po + ((row0 + Ro) * NS + split) * D;
        for (int d0 = 0; d0 < 4; ++d0) prow[d0 * 32 + r32] = o[d0][r]; } }
  }
}

constexpr int CMB_ROWS = 8, CMB_BLOCKS = (int)(((size_t)B * HQ * SQ + CMB_ROWS - 1) / CMB_ROWS);
template <int NS> __global__ __launch_bounds__(256) void gqa_chunk_combine(const float* __restrict__ pml, const float* __restrict__ po, float* __restrict__ O) {
  using u32x2 = __attribute__((ext_vector_type(2))) unsigned;
  const size_t row = (size_t)blockIdx.x * CMB_ROWS + (threadIdx.x >> 5); const int d = (threadIdx.x & 31) * 4;
  if (row >= (size_t)B * HQ * SQ) return;
  const float* ml = pml + row * NS * 2; const float* acc = po + row * NS * D + d;
  float M = -__builtin_inff(); for (int s = 0; s < NS; ++s) M = fmaxf(M, ml[2 * s]);
  const float Msub = (M == -__builtin_inff()) ? 0.f : M; float L = 0.f; f32x4 o = {0.f, 0.f, 0.f, 0.f};
  for (int s = 0; s < NS; ++s) { const float m = ml[2 * s], l = ml[2 * s + 1]; const float w = l > 0.f ? __builtin_amdgcn_exp2f(m - Msub) : 0.f;
    const f32x4 a = *reinterpret_cast<const f32x4*>(acc + (size_t)s * D); L += w * l; o[0] += w * a[0]; o[1] += w * a[1]; o[2] += w * a[2]; o[3] += w * a[3]; }
  const float inv = L > 0.f ? 1.f / L : 0.f; o[0] *= inv; o[1] *= inv; o[2] *= inv; o[3] *= inv;
  u32x2 w2 = {cvtpk(o[0], o[1]), cvtpk(o[2], o[3])};
  *reinterpret_cast<u32x2*>((unsigned short*)O + row * D + d) = w2;
}

__global__ __launch_bounds__(256) void cvt_f32_bf16(const float* __restrict__ q, const float* __restrict__ k, const float* __restrict__ v, bf16* __restrict__ qb, bf16* __restrict__ kb, bf16* __restrict__ vb) {
  const size_t i0 = ((size_t)blockIdx.x * 256 + threadIdx.x) * 8, step = (size_t)gridDim.x * 256 * 8;
  for (size_t i = i0; i < NQ; i += step) *reinterpret_cast<bf16x8*>(qb + i) = ld8(q + i);
  for (size_t i = i0; i < NKV; i += step) { *reinterpret_cast<bf16x8*>(kb + i) = ld8(k + i); *reinterpret_cast<bf16x8*>(vb + i) = ld8(v + i); }
}
template <class TQ, class TKV>
static void gqa_chunk_forward(const TQ* q, const TKV* k, const TKV* v, const int* lens, float* o, void* ws, hipStream_t stream) {
  constexpr size_t shm = shm_bytes<TKV>();
  (void)hipFuncSetAttribute((const void*)gqa_chunk_fwd<TQ, TKV>, hipFuncAttributeMaxDynamicSharedMemorySize, (int)shm);
  float* pml = (float*)((char*)ws + WS_PML); float* po = (float*)((char*)ws + WS_PO);
  constexpr int NS = ns_for<TKV>();
  hipLaunchKernelGGL((gqa_chunk_fwd<TQ, TKV>), dim3(NS, HKV, B), dim3(64 * nw_for<TKV>()), shm, stream, q, k, v, lens, o, pml, po);
  if constexpr (NS > 1) hipLaunchKernelGGL((gqa_chunk_combine<NS>), dim3(CMB_BLOCKS), dim3(256), 0, stream, (const float*)pml, (const float*)po, o);
  const hipError_t le = hipPeekAtLastError();
  if (le != hipSuccess) fprintf(stderr, "kernel_launch: launch failed: %s (grid %d x %d x %d, %zu B LDS)\n", hipGetErrorName(le), NS, HKV, B, shm);
}

extern "C" void kernel_launch(void* const* d_in, const int* in_sizes, int n_in, void* d_out, int out_size, void* d_ws, size_t ws_size, hipStream_t stream) {
  gqa_chunk_forward<bf16, bf16>((const bf16*)d_in[0], (const bf16*)d_in[1], (const bf16*)d_in[2], nullptr, (float*)d_out, d_ws, stream);
}
```

```cpp
#include <hip/hip_runtime.h>
#include <hip/hip_bf16.h>

__global__ __launch_bounds__(256, 1) void gqa_attn_kernel(const void* q, const void* k, const void* v, void* out, void* ws) {
  const unsigned wave = __builtin_amdgcn_readfirstlane(threadIdx.x >> 6);
  asm volatile(
      "s_load_dwordx8 s[4:11], %0, 0x0\n"
      "s_load_dwordx2 s[12:13], %0, 0x20\n"
      "s_mov_b32 s36, %2\n"
      "s_and_b32 s52, %1, 7\n"
      "s_lshr_b32 s53, %1, 3\n"
      "s_lshr_b32 s38, s53, 2\n"
      "s_lshl_b32 s38, s38, 3\n"
      "s_add_u32 s38, s38, s52\n"
      "s_and_b32 s39, s53, 1\n"
      "s_bfe_u32 s37, s53, 0x10001\n"
      "v_mbcnt_lo_u32_b32 v11, -1, 0\n"
      "v_mbcnt_hi_u32_b32 v11, -1, v11\n"
      "s_cmp_lt_u32 %1, 8\n"
      "s_cbranch_scc0 NOPW_%=\n"
      "s_getpc_b64 s[16:17]\n"
      "PCW_%=:\n"
      "s_and_b32 s17, s17, 0xffff\n"
      "s_mov_b32 s18, CODEEND_%=-PCW_%=\n"
      "s_mov_b32 s19, 0x20000\n"
      "s_lshl_b32 s52, s36, 12\n"
      "v_lshlrev_b32 v26, 6, v11\n"
      "buffer_load_dword v0, v26, s[16:19], s52 offen\n"
      "s_add_u32 s52, s52, 0x4000\n"
      "buffer_load_dword v0, v26, s[16:19], s52 offen\n"
      "NOPW_%=:\n"
      "v_bfe_u32 v20, v11, 2, 3\n"
      "v_and_b32 v21, 3, v11\n"
      "v_lshrrev_b32 v22, 5, v11\n"
      "v_lshrrev_b32 v23, 2, v20\n"
      "v_xor_b32 v21, v21, v23\n"
      "v_lshlrev_b32 v20, 8, v20\n"
      "v_lshl_add_u32 v20, v21, 4, v20\n"
      "s_and_b32 s52, s36, 1\n"
      "s_lshl_b32 s52, s52, 5\n"
      "v_xor_b32 v24, s52, v20\n"
      "v_lshl_add_u32 v24, v22, 6, v24\n"
      "v_xor_b32 v23, s36, v22\n"
      "v_lshlrev_b32 v23, 6, v23\n"
      "v_xor_b32 v26, 0x80, v23\n"
      "v_xor_b32 v21, 32, v20\n"
      "s_mov_b32 s51, 0\n"
      "s_and_b32 s54, s51, 1\n"
      "s_lshl_b32 s54, s54, 11\n"
      "s_add_u32 s53, s54, 0xc00\n"
      "v_add3_u32 v1, v20, v23, s53\n"
      "s_add_u32 s53, s54, 0x800\n"
      "v_add3_u32 v2, v20, v26, s53\n"
      "s_sub_u32 s53, 0x2400, s54\n"
      "v_add3_u32 v3, v21, v23, s53\n"
      "s_sub_u32 s53, 0x2000, s54\n"
      "v_add3_u32 v4, v21, v26, s53\n"
      "s_mul_i32 s52, s38, 49152\n"
      "s_mul_i32 s53, s39, 24576\n"
      "s_add_u32 s50, s52, s53\n"
      "s_lshl_b32 s52, s38, 19\n"
      "s_lshl_b32 s53, s37, 18\n"
      "s_add_u32 s55, s52, s53\n"
      "s_sub_u32 s55, s55, 0xc00\n"
      "s_ashr_i32 s45, s55, 31\n"
      "s_mov_b32 s30, 24576\n"
      "s_mov_b32 s31, 0x20000\n"
      "s_mov_b32 s22, 265216\n"
      "s_mov_b32 s23, 0x20000\n"
      "s_mov_b32 s26, 265216\n"
      "s_mov_b32 s27, 0x20000\n"
      "s_lshl_b32 s41, s36, 13\n"
      "s_lshr_b32 s54, s51, 2\n"
      "s_xor_b32 s54, s54, s36\n"
      "s_lshl_b32 s65, s54, 11\n"
      "s_and_b32 s54, s51, 2\n"
      "s_lshl_b32 s54, s54, 11\n"
      "s_add_u32 s65, s65, s54\n"
      "s_mov_b32 s63, s65\n"
      "s_xor_b32 s64, s63, 0x4000\n"
      "s_lshl_b32 s40, s36, 15\n"
      "s_mov_b32 m0, s40\n"
      "s_add_u32 s41, s41, 131072\n"
      "s_lshl_b32 s60, s36, 11\n"
      "s_add_u32 s61, s60, 0x2000\n"
      "s_add_u32 s62, s60, 0x4000\n"
      "v_lshlrev_b32 v14, 4, v11\n"
      "v_add_u32 v25, s41, v14\n"
      "s_mov_b32 s34, 0x3e0293ee\n"
      "s_mov_b32 s35, s34\n"
      "s_waitcnt lgkmcnt(0)\n"
      "s_add_u32 s28, s4, s50\n"
      "s_addc_u32 s29, s5, 0\n"
      "s_and_b32 s29, s29, 0xffff\n"
      "buffer_load_dwordx4 v[32:35], v24, s[28:31], s60 offen nt\n"
      "buffer_load_dwordx4 v[36:39], v24, s[28:31], s60 offen offset:128 nt\n"
      "buffer_load_dwordx4 v[40:43], v24, s[28:31], s61 offen nt\n"
      "buffer_load_dwordx4 v[44:47], v24, s[28:31], s61 offen offset:128 nt\n"
      "buffer_load_dwordx4 v[48:51], v24, s[28:31], s62 offen nt\n"
      "buffer_load_dwordx4 v[52:55], v24, s[28:31], s62 offen offset:128 nt\n"
      "s_add_u32 s20, s6, s55\n"
      "s_addc_u32 s21, s7, s45\n"
      "s_and_b32 s21, s21, 0xffff\n"
      "buffer_load_dwordx4 v1, s[20:23], s63 offen offset:0 nt lds\n"
      "buffer_load_dwordx4 v2, s[20:23], s63 offen offset:1024 nt lds\n"
      "buffer_load_dwordx4 v3, s[20:23], s63 offen offset:2048 nt lds\n"
      "buffer_load_dwordx4 v4, s[20:23], s63 offen offset:3072 nt lds\n"
      "s_add_u32 s24, s8, s55\n"
      "s_addc_u32 s25, s9, s45\n"
      "s_and_b32 s25, s25, 0xffff\n"
      "v_bfe_u32 v20, v11, 2, 2\n"
      "v_lshrrev_b32 v23, 4, v11\n"
      "v_and_b32 v21, 1, v20\n"
      "v_lshlrev_b32 v21, 1, v21\n"
      "v_xor_b32 v21, v21, v23\n"
      "v_lshlrev_b32 v21, 4, v21\n"
      "v_and_b32 v22, 3, v11\n"
      "v_lshl_add_u32 v21, v22, 6, v21\n"
      "v_lshl_add_u32 v22, v20, 11, v21\n"
      "v_add_u32 v5, s40, v22\n"
      "v_xor_b32 v6, 0x110, v5\n"
      "v_lshl_add_u32 v22, v20, 13, v21\n"
      "s_add_u32 m0, s40, 0x1000\n"
      "s_nop 0\n"
      "buffer_load_dwordx4 v1, s[20:23], s64 offen offset:0 nt lds\n"
      "v_add_u32 v22, 131072, v22\n"
      "s_xor_b32 s53, s36, 0\n"
      "s_lshl_b32 s53, s53, 9\n"
      "v_add_u32 v64, s53, v22\n"
      "v_xor_b32 v65, 0x110, v64\n"
      "s_xor_b32 s53, s36, 1\n"
      "s_lshl_b32 s53, s53, 9\n"
      "v_add_u32 v66, s53, v22\n"
      "v_xor_b32 v67, 0x110, v66\n"
      "s_xor_b32 s53, s36, 2\n"
      "s_lshl_b32 s53, s53, 9\n"
      "v_add_u32 v68, s53, v22\n"
      "buffer_load_dwordx4 v2, s[20:23], s64 offen offset:1024 nt lds\n"
      "v_xor_b32 v69, 0x110, v68\n"
      "s_xor_b32 s53, s36, 3\n"
      "s_lshl_b32 s53, s53, 9\n"
      "v_add_u32 v70, s53, v22\n"
      "v_xor_b32 v71, 0x110, v70\n"
      "v_and_b32 v21, 1, v23\n"
      "v_bfe_u32 v22, v11, 1, 1\n"
      "v_lshl_add_u32 v21, v21, 1, v22\n"
      "v_lshlrev_b32 v21, 4, v21\n"
      "v_lshl_add_u32 v21, v23, 11, v21\n"
      "v_lshl_add_u32 v21, v20, 6, v21\n"
      "v_and_b32 v22, 1, v11\n"
      "buffer_load_dwordx4 v3, s[20:23], s64 offen offset:2048 nt lds\n"
      "v_lshl_add_u32 v21, v22, 3, v21\n"
      "s_add_u32 s52, s40, 0x2000\n"
      "v_add_u32 v7, s52, v21\n"
      "v_xor_b32 v8, 0x110, v7\n"
      "v_xor_b32 v9, 0x20, v7\n"
      "v_xor_b32 v10, 0x130, v7\n"
      "s_mov_b32 s43, 0\n"
      "v_mov_b32 v104, 0\n"
      "v_mov_b32 v105, 0\n"
      "v_mov_b32 v106, 0\n"
      "v_mov_b32 v107, 0\n"
      "v_mov_b32 v108, 0\n"
      "buffer_load_dwordx4 v4, s[20:23], s64 offen offset:3072 nt lds\n"
      "v_mov_b32 v109, 0\n"
      "v_mov_b32 v110, 0\n"
      "v_mov_b32 v111, 0\n"
      "s_waitcnt vmcnt(13)\n"
      "v_lshlrev_b32 v56, 16, v32\n"
      "v_and_b32 v57, 0xffff0000, v32\n"
      "v_pk_mul_f32 v[56:57], v[56:57], s[34:35]\n"
      "v_cvt_pk_bf16_f32 v32, v56, v57\n"
      "v_lshlrev_b32 v58, 16, v33\n"
      "v_and_b32 v59, 0xffff0000, v33\n"
      "v_pk_mul_f32 v[58:59], v[58:59], s[34:35]\n"
      "v_cvt_pk_bf16_f32 v33, v58, v59\n"
      "s_add_u32 m0, s40, 0x2000\n"
      "s_nop 0\n"
      "buffer_load_dwordx4 v1, s[24:27], s63 offen offset:0 nt lds\n"
      "v_lshlrev_b32 v56, 16, v34\n"
      "v_and_b32 v57, 0xffff0000, v34\n"
      "v_pk_mul_f32 v[56:57], v[56:57], s[34:35]\n"
      "v_cvt_pk_bf16_f32 v34, v56, v57\n"
      "v_lshlrev_b32 v58, 16, v35\n"
      "v_and_b32 v59, 0xffff0000, v35\n"
      "v_pk_mul_f32 v[58:59], v[58:59], s[34:35]\n"
      "v_cvt_pk_bf16_f32 v35, v58, v59\n"
      "ds_write_b128 v25, v[32:35] offset:0\n"
      "s_waitcnt vmcnt(13)\n"
      "v_lshlrev_b32 v56, 16, v36\n"
      "v_and_b32 v57, 0xffff0000, v36\n"
      "buffer_load_dwordx4 v2, s[24:27], s63 offen offset:1024 nt lds\n"
      "v_pk_mul_f32 v[56:57], v[56:57], s[34:35]\n"
      "v_cvt_pk_bf16_f32 v36, v56, v57\n"
      "v_lshlrev_b32 v58, 16, v37\n"
      "v_and_b32 v59, 0xffff0000, v37\n"
      "v_pk_mul_f32 v[58:59], v[58:59], s[34:35]\n"
      "v_cvt_pk_bf16_f32 v37, v58, v59\n"
      "v_lshlrev_b32 v56, 16, v38\n"
      "v_and_b32 v57, 0xffff0000, v38\n"
      "v_pk_mul_f32 v[56:57], v[56:57], s[34:35]\n"
      "v_cvt_pk_bf16_f32 v38, v56, v57\n"
      "v_lshlrev_b32 v58, 16, v39\n"
      "v_and_b32 v59, 0xffff0000, v39\n"
      "buffer_load_dwordx4 v3, s[24:27], s63 offen offset:2048 nt lds\n"
      "v_pk_mul_f32 v[58:59], v[58:59], s[34:35]\n"
      "v_cvt_pk_bf16_f32 v39, v58, v59\n"
      "ds_write_b128 v25, v[36:39] offset:1024\n"
      "s_waitcnt vmcnt(14)\n"
      "v_lshlrev_b32 v56, 16, v40\n"
      "v_and_b32 v57, 0xffff0000, v40\n"
      "v_pk_mul_f32 v[56:57], v[56:57], s[34:35]\n"
      "v_cvt_pk_bf16_f32 v40, v56, v57\n"
      "v_lshlrev_b32 v58, 16, v41\n"
      "v_and_b32 v59, 0xffff0000, v41\n"
      "v_pk_mul_f32 v[58:59], v[58:59], s[34:35]\n"
      "v_cvt_pk_bf16_f32 v41, v58, v59\n"
      "buffer_load_dwordx4 v4, s[24:27], s63 offen offset:3072 nt lds\n"
      "v_lshlrev_b32 v56, 16, v42\n"
      "v_and_b32 v57, 0xffff0000, v42\n"
      "v_pk_mul_f32 v[56:57], v[56:57], s[34:35]\n"
      "v_cvt_pk_bf16_f32 v42, v56, v57\n"
      "v_lshlrev_b32 v58, 16, v43\n"
      "v_and_b32 v59, 0xffff0000, v43\n"
      "v_pk_mul_f32 v[58:59], v[58:59], s[34:35]\n"
      "v_cvt_pk_bf16_f32 v43, v58, v59\n"
      "ds_write_b128 v25, v[40:43] offset:2048\n"
      "s_waitcnt vmcnt(14)\n"
      "v_lshlrev_b32 v56, 16, v44\n"
      "v_and_b32 v57, 0xffff0000, v44\n"
      "v_pk_mul_f32 v[56:57], v[56:57], s[34:35]\n"
      "v_cvt_pk_bf16_f32 v44, v56, v57\n"
      "v_lshlrev_b32 v58, 16, v45\n"
      "v_and_b32 v59, 0xffff0000, v45\n"
      "v_pk_mul_f32 v[58:59], v[58:59], s[34:35]\n"
      "v_cvt_pk_bf16_f32 v45, v58, v59\n"
      "v_lshlrev_b32 v56, 16, v46\n"
      "v_and_b32 v57, 0xffff0000, v46\n"
      "v_pk_mul_f32 v[56:57], v[56:57], s[34:35]\n"
      "v_cvt_pk_bf16_f32 v46, v56, v57\n"
      "v_lshlrev_b32 v58, 16, v47\n"
      "v_and_b32 v59, 0xffff0000, v47\n"
      "v_pk_mul_f32 v[58:59], v[58:59], s[34:35]\n"
      "v_cvt_pk_bf16_f32 v47, v58, v59\n"
      "ds_write_b128 v25, v[44:47] offset:3072\n"
      "s_waitcnt vmcnt(13)\n"
      "v_lshlrev_b32 v56, 16, v48\n"
      "v_and_b32 v57, 0xffff0000, v48\n"
      "v_pk_mul_f32 v[56:57], v[56:57], s[34:35]\n"
      "v_cvt_pk_bf16_f32 v48, v56, v57\n"
      "v_lshlrev_b32 v58, 16, v49\n"
      "v_and_b32 v59, 0xffff0000, v49\n"
      "v_pk_mul_f32 v[58:59], v[58:59], s[34:35]\n"
      "v_cvt_pk_bf16_f32 v49, v58, v59\n"
      "v_lshlrev_b32 v56, 16, v50\n"
      "v_and_b32 v57, 0xffff0000, v50\n"
      "v_pk_mul_f32 v[56:57], v[56:57], s[34:35]\n"
      "v_cvt_pk_bf16_f32 v50, v56, v57\n"
      "v_lshlrev_b32 v58, 16, v51\n"
      "v_and_b32 v59, 0xffff0000, v51\n"
      "v_pk_mul_f32 v[58:59], v[58:59], s[34:35]\n"
      "v_cvt_pk_bf16_f32 v51, v58, v59\n"
      "ds_write_b128 v25, v[48:51] offset:4096\n"
      "s_waitcnt vmcnt(12)\n"
      "v_lshlrev_b32 v56, 16, v52\n"
      "v_and_b32 v57, 0xffff0000, v52\n"
      "v_pk_mul_f32 v[56:57], v[56:57], s[34:35]\n"
      "v_cvt_pk_bf16_f32 v52, v56, v57\n"
      "v_lshlrev_b32 v58, 16, v53\n"
      "v_and_b32 v59, 0xffff0000, v53\n"
      "v_pk_mul_f32 v[58:59], v[58:59], s[34:35]\n"
      "v_cvt_pk_bf16_f32 v53, v58, v59\n"
      "v_lshlrev_b32 v56, 16, v54\n"
      "v_and_b32 v57, 0xffff0000, v54\n"
      "v_pk_mul_f32 v[56:57], v[56:57], s[34:35]\n"
      "v_cvt_pk_bf16_f32 v54, v56, v57\n"
      "v_lshlrev_b32 v58, 16, v55\n"
      "v_and_b32 v59, 0xffff0000, v55\n"
      "v_pk_mul_f32 v[58:59], v[58:59], s[34:35]\n"
      "v_cvt_pk_bf16_f32 v55, v58, v59\n"
      "ds_write_b128 v25, v[52:55] offset:5120\n"
      "s_waitcnt lgkmcnt(0)\n"
      "s_barrier\n"
      "s_cmp_eq_u32 s37, 0\n"
      "s_cbranch_scc1 QK0_%=\n"
      "ds_read_b128 v[160:163], v64 offset:0\n"
      "ds_read_b128 v[164:167], v66 offset:0\n"
      "ds_read_b128 v[168:171], v68 offset:0\n"
      "ds_read_b128 v[172:175], v70 offset:0\n"
      "ds_read_b128 v[176:179], v65 offset:0\n"
      "ds_read_b128 v[180:183], v67 offset:0\n"
      "ds_read_b128 v[184:187], v69 offset:0\n"
      "ds_read_b128 v[188:191], v71 offset:0\n"
      "ds_read_b128 v[192:195], v64 offset:2048\n"
      "ds_read_b128 v[196:199], v66 offset:2048\n"
      "ds_read_b128 v[200:203], v68 offset:2048\n"
      "ds_read_b128 v[204:207], v70 offset:2048\n"
      "ds_read_b128 v[208:211], v65 offset:2048\n"
      "ds_read_b128 v[212:215], v67 offset:2048\n"
      "ds_read_b128 v[216:219], v69 offset:2048\n"
      "ds_read_b128 v[220:223], v71 offset:2048\n"
      "ds_read_b128 v[224:227], v64 offset:4096\n"
      "ds_read_b128 v[228:231], v66 offset:4096\n"
      "ds_read_b128 v[232:235], v68 offset:4096\n"
      "ds_read_b128 v[236:239], v70 offset:4096\n"
      "ds_read_b128 v[240:243], v65 offset:4096\n"
      "ds_read_b128 v[244:247], v67 offset:4096\n"
      "ds_read_b128 v[248:251], v69 offset:4096\n"
      "ds_read_b128 v[252:255], v71 offset:4096\n"
      "s_branch QKD_%=\n"
      "QK0_%=:\n"
      "ds_read_b128 v[160:163], v65 offset:2048\n"
      "ds_read_b128 v[164:167], v67 offset:2048\n"
      "ds_read_b128 v[168:171], v69 offset:2048\n"
      "ds_read_b128 v[172:175], v71 offset:2048\n"
      "ds_read_b128 v[176:179], v64 offset:4096\n"
      "ds_read_b128 v[180:183], v66 offset:4096\n"
      "ds_read_b128 v[184:187], v68 offset:4096\n"
      "ds_read_b128 v[188:191], v70 offset:4096\n"
      "ds_read_b128 v[192:195], v65 offset:4096\n"
      "ds_read_b128 v[196:199], v67 offset:4096\n"
      "ds_read_b128 v[200:203], v69 offset:4096\n"
      "ds_read_b128 v[204:207], v71 offset:4096\n"
      "ds_read_b128 v[208:211], v64 offset:0\n"
      "ds_read_b128 v[212:215], v66 offset:0\n"
      "ds_read_b128 v[216:219], v68 offset:0\n"
      "ds_read_b128 v[220:223], v70 offset:0\n"
      "ds_read_b128 v[224:227], v65 offset:0\n"
      "ds_read_b128 v[228:231], v67 offset:0\n"
      "ds_read_b128 v[232:235], v69 offset:0\n"
      "ds_read_b128 v[236:239], v71 offset:0\n"
      "ds_read_b128 v[240:243], v64 offset:2048\n"
      "ds_read_b128 v[244:247], v66 offset:2048\n"
      "ds_read_b128 v[248:251], v68 offset:2048\n"
      "ds_read_b128 v[252:255], v70 offset:2048\n"
      "QKD_%=:\n"
      "s_waitcnt vmcnt(4)\n"
      "ds_read_b128 a[192:195], v5 offset:0\n"
      "ds_read_b128 a[196:199], v6 offset:0\n"
      "ds_read_b128 a[200:203], v5 offset:512\n"
      "ds_read_b128 a[204:207], v6 offset:512\n"
      "ds_read_b128 a[208:211], v5 offset:1024\n"
      "ds_read_b128 a[212:215], v6 offset:1024\n"
      "ds_read_b128 a[216:219], v5 offset:1536\n"
      "ds_read_b128 a[220:223], v6 offset:1536\n"
      "s_waitcnt lgkmcnt(7)\n"
      "v_mfma_f32_16x16x32_bf16 v[32:35], a[192:195], v[160:163], v[104:107]\n"
      "s_add_u32 m0, s40, 0x3000\n"
      "s_mov_b32 s16, s12\n"
      "s_and_b32 s17, s13, 0xffff\n"
      "v_mfma_f32_16x16x32_bf16 v[40:43], a[192:195], v[176:179], v[108:111]\n"
      "buffer_load_dwordx4 v1, s[24:27], s64 offen offset:0 nt lds\n"
      "buffer_load_dwordx4 v2, s[24:27], s64 offen offset:1024 nt lds\n"
      "s_mov_b32 s18, 8519680\n"
      "s_mov_b32 s19, 0x20000\n"
      "s_lshl_b32 s52, s38, 1\n"
      "s_waitcnt lgkmcnt(6)\n"
      "v_mfma_f32_16x16x32_bf16 v[36:39], a[196:199], v[160:163], v[104:107]\n"
      "buffer_load_dwordx4 v3, s[24:27], s64 offen offset:2048 nt lds\n"
      "buffer_load_dwordx4 v4, s[24:27], s64 offen offset:3072 nt lds\n"
      "s_add_u32 s52, s52, s39\n"
      "s_lshl_b32 s52, s52, 1\n"
      "s_add_u32 s52, s52, s37\n"
      "v_mfma_f32_16x16x32_bf16 v[44:47], a[196:199], v[176:179], v[108:111]\n"
      "s_add_u32 m0, s40, 0x4000\n"
      "s_add_u32 s63, s63, 0x8000\n"
      "s_lshl_b32 s52, s52, 2\n"
      "s_add_u32 s52, s52, s36\n"
      "s_lshl_b32 s48, s52, 7\n"
      "s_waitcnt lgkmcnt(0)\n"
      "v_mfma_f32_16x16x32_bf16 v[32:35], a[200:203], v[164:167], v[32:35]\n"
      "buffer_load_dwordx4 v1, s[20:23], s63 offen offset:0 nt lds\n"
      "buffer_load_dwordx4 v2, s[20:23], s63 offen offset:1024 nt lds\n"
      "s_xor_b32 s49, s48, 0x200\n"
      "s_lshl_b32 s46, s52, 12\n"
      "s_add_u32 s46, s46, 131072\n"
      "v_mfma_f32_16x16x32_bf16 v[40:43], a[200:203], v[180:183], v[40:43]\n"
      "buffer_load_dwordx4 v3, s[20:23], s63 offen offset:2048 nt lds\n"
      "buffer_load_dwordx4 v4, s[20:23], s63 offen offset:3072 nt lds\n"
      "s_xor_b32 s47, s46, 0x4000\n"
      "buffer_load_dword v159, off, s[16:19], s48 sc1\n"
      "v_mfma_f32_16x16x32_bf16 v[36:39], a[204:207], v[164:167], v[36:39]\n"
      "s_add_u32 m0, s40, 0x5000\n"
      "s_add_u32 s64, s64, 0x8000\n"
      "buffer_load_dword v19, off, s[16:19], s49 offset:4 sc1\n"
      "s_mov_b32 s42, 0x53800000\n"
      "s_mov_b32 s56, 0xffff0000\n"
      "v_mfma_f32_16x16x32_bf16 v[44:47], a[204:207], v[180:183], v[44:47]\n"
      "buffer_load_dwordx4 v1, s[20:23], s64 offen offset:0 nt lds\n"
      "buffer_load_dwordx4 v2, s[20:23], s64 offen offset:1024 nt lds\n"
      "s_mov_b32 s57, 0\n"
      "s_mov_b32 s58, 0\n"
      "s_mov_b32 s59, -1\n"
      "v_mfma_f32_16x16x32_bf16 v[32:35], a[208:211], v[168:171], v[32:35]\n"
      "buffer_load_dwordx4 v3, s[20:23], s64 offen offset:2048 nt lds\n"
      "buffer_load_dwordx4 v4, s[20:23], s64 offen offset:3072 nt lds\n"
      "v_mov_b32 v112, 0\n"
      "v_mov_b32 v113, 0\n"
      "v_mov_b32 v114, 0\n"
      "v_mfma_f32_16x16x32_bf16 v[40:43], a[208:211], v[184:187], v[40:43]\n"
      "s_add_u32 m0, s40, 0x6000\n"
      "v_mov_b32 v115, 0\n"
      "v_mov_b32 v116, 0\n"
      "v_mov_b32 v117, 0\n"
      "v_mfma_f32_16x16x32_bf16 v[36:39], a[212:215], v[168:171], v[36:39]\n"
      "buffer_load_dwordx4 v1, s[24:27], s63 offen offset:0 nt lds\n"
      "buffer_load_dwordx4 v2, s[24:27], s63 offen offset:1024 nt lds\n"
      "v_mov_b32 v118, 0\n"
      "v_mov_b32 v119, 0\n"
      "v_mfma_f32_16x16x32_bf16 v[44:47], a[212:215], v[184:187], v[44:47]\n"
      "buffer_load_dwordx4 v3, s[24:27], s63 offen offset:2048 nt lds\n"
      "buffer_load_dwordx4 v4, s[24:27], s63 offen offset:3072 nt lds\n"
      "v_mov_b32 v120, 0\n"
      "v_mov_b32 v121, 0\n"
      "v_mov_b32 v122, 0\n"
      "v_mfma_f32_16x16x32_bf16 v[32:35], a[216:219], v[172:175], v[32:35]\n"
      "s_add_u32 m0, s40, 0x7000\n"
      "v_mov_b32 v123, 0\n"
      "v_mov_b32 v124, 0\n"
      "v_mov_b32 v125, 0\n"
      "v_mfma_f32_16x16x32_bf16 v[40:43], a[216:219], v[188:191], v[40:43]\n"
      "buffer_load_dwordx4 v1, s[24:27], s64 offen offset:0 nt lds\n"
      "buffer_load_dwordx4 v2, s[24:27], s64 offen offset:1024 nt lds\n"
      "v_mov_b32 v126, 0\n"
      "v_mov_b32 v127, 0\n"
      "v_mov_b32 v152, 0x3f803f80\n"
      "v_mfma_f32_16x16x32_bf16 v[36:39], a[220:223], v[172:175], v[36:39]\n"
      "buffer_load_dwordx4 v3, s[24:27], s64 offen offset:2048 nt lds\n"
      "buffer_load_dwordx4 v4, s[24:27], s64 offen offset:3072 nt lds\n"
      "v_mov_b32 v153, 0x3f803f80\n"
      "v_mov_b32 v154, 0x3f803f80\n"
      "v_mov_b32 v155, 0x3f803f80\n"
      "v_mfma_f32_16x16x32_bf16 v[44:47], a[220:223], v[188:191], v[44:47]\n"
      "v_mov_b32 v12, 0\n"
      "v_mov_b32 v13, 0\n"
      "v_mov_b32 v15, 0x2b800000\n"
      "ENTRY_%=:\n"
      "s_mov_b32 m0, s40\n"
      "s_add_u32 s63, s63, 0x8000\n"
      "v_exp_f32 v20, v32\n"
      "v_exp_f32 v21, v33\n"
      "v_exp_f32 v22, v34\n"
      "v_mfma_f32_16x16x32_bf16 v[48:51], a[192:195], v[192:195], v[112:115]\n"
      "s_waitcnt vmcnt(16)\n"
      "v_mfma_f32_16x16x32_bf16 v[56:59], a[192:195], v[208:211], v[116:119]\n"
      "v_exp_f32 v23, v35\n"
      "v_mfma_f32_16x16x32_bf16 v[52:55], a[196:199], v[192:195], v[112:115]\n"
      "buffer_load_dwordx4 v1, s[20:23], s63 offen offset:0 nt lds\n"
      "v_mfma_f32_16x16x32_bf16 v[60:63], a[196:199], v[208:211], v[116:119]\n"
      "v_exp_f32 v24, v36\n"
      "s_nop 7\n"
      "v_mfma_f32_16x16x32_bf16 v[48:51], a[200:203], v[196:199], v[48:51]\n"
      "v_exp_f32 v25, v37\n"
      "v_mfma_f32_16x16x32_bf16 v[56:59], a[200:203], v[212:215], v[56:59]\n"
      "ds_read_b64_tr_b16 a[224:225], v7 offset:0\n"
      "v_cvt_pk_bf16_f32 v80, v20, v21\n"
      "v_mfma_f32_16x16x32_bf16 v[52:55], a[204:207], v[196:199], v[52:55]\n"
      "v_exp_f32 v156, v38\n"
      "v_mfma_f32_16x16x32_bf16 v[60:63], a[204:207], v[212:215], v[60:63]\n"
      "v_exp_f32 v157, v39\n"
      "s_nop 7\n"
      "v_mfma_f32_16x16x32_bf16 v[48:51], a[208:211], v[200:203], v[48:51]\n"
      "ds_read_b64_tr_b16 a[226:227], v8 offset:0\n"
      "v_cvt_pk_bf16_f32 v81, v22, v23\n"
      "v_mfma_f32_16x16x32_bf16 v[56:59], a[208:211], v[216:219], v[56:59]\n"
      "buffer_load_dwordx4 v2, s[20:23], s63 offen offset:1024 nt lds\n"
      "ds_read_b64_tr_b16 a[228:229], v9 offset:0\n"
      "ds_read_b64_tr_b16 a[230:231], v10 offset:0\n"
      "v_mfma_f32_16x16x32_bf16 v[52:55], a[212:215], v[200:203], v[52:55]\n"
      "ds_read_b64_tr_b16 a[232:233], v7 offset:512\n"
      "v_exp_f32 v20, v40\n"
      "v_mfma_f32_16x16x32_bf16 v[60:63], a[212:215], v[216:219], v[60:63]\n"
      "ds_read_b64_tr_b16 a[234:235], v8 offset:512\n"
      "v_cvt_pk_bf16_f32 v82, v24, v25\n"
      "v_exp_f32 v21, v41\n"
      "s_nop 7\n"
      "v_mfma_f32_16x16x32_bf16 v[48:51], a[216:219], v[204:207], v[48:51]\n"
      "ds_read_b64_tr_b16 a[236:237], v9 offset:512\n"
      "ds_read_b64_tr_b16 a[238:239], v10 offset:512\n"
      "v_exp_f32 v22, v42\n"
      "v_mfma_f32_16x16x32_bf16 v[56:59], a[216:219], v[220:223], v[56:59]\n"
      "ds_read_b64_tr_b16 a[240:241], v7 offset:1024\n"
      "ds_read_b64_tr_b16 a[242:243], v8 offset:1024\n"
      "ds_read_b64_tr_b16 a[244:245], v9 offset:1024\n"
      "ds_read_b64_tr_b16 a[246:247], v10 offset:1024\n"
      "v_mfma_f32_16x16x32_bf16 v[52:55], a[220:223], v[204:207], v[52:55]\n"
      "ds_read_b64_tr_b16 a[248:249], v7 offset:1536\n"
      "v_cvt_pk_bf16_f32 v83, v156, v157\n"
      "v_exp_f32 v23, v43\n"
      "v_mfma_f32_16x16x32_bf16 v[60:63], a[220:223], v[220:223], v[60:63]\n"
      "ds_read_b64_tr_b16 a[250:251], v8 offset:1536\n"
      "buffer_load_dwordx4 v3, s[20:23], s63 offen offset:2048 nt lds\n"
      "v_exp_f32 v24, v44\n"
      "s_waitcnt lgkmcnt(6)\n"
      "v_mfma_f32_16x16x32_bf16 v[128:131], v[152:155], v[80:83], 0\n"
      "ds_read_b64_tr_b16 a[252:253], v9 offset:1536\n"
      "v_cvt_pk_bf16_f32 v84, v20, v21\n"
      "s_nop 7\n"
      "v_mfma_f32_16x16x32_bf16 a[0:3], a[224:227], v[80:83], 0\n"
      "ds_read_b64_tr_b16 a[254:255], v10 offset:1536\n"
      "v_exp_f32 v25, v45\n"
      "v_mfma_f32_16x16x32_bf16 a[4:7], a[228:231], v[80:83], 0\n"
      "v_exp_f32 v156, v46\n"
      "v_mfma_f32_16x16x32_bf16 a[8:11], a[232:235], v[80:83], 0\n"
      "v_exp_f32 v157, v47\n"
      "v_mfma_f32_16x16x32_bf16 a[12:15], a[236:239], v[80:83], 0\n"
      "s_waitcnt lgkmcnt(0)\n"
      "s_nop 7\n"
      "v_mfma_f32_16x16x32_bf16 a[16:19], a[240:243], v[80:83], 0\n"
      "v_cvt_pk_bf16_f32 v85, v22, v23\n"
      "v_cvt_pk_bf16_f32 v86, v24, v25\n"
      "v_mfma_f32_16x16x32_bf16 a[20:23], a[244:247], v[80:83], 0\n"
      "buffer_load_dwordx4 v4, s[20:23], s63 offen offset:3072 nt lds\n"
      "v_mfma_f32_16x16x32_bf16 a[24:27], a[248:251], v[80:83], 0\n"
      "s_add_u32 m0, s40, 0x1000\n"
      "s_add_u32 s64, s64, 0x8000\n"
      "v_mfma_f32_16x16x32_bf16 a[28:31], a[252:255], v[80:83], 0\n"
      "v_cvt_pk_bf16_f32 v87, v156, v157\n"
      "v_exp_f32 v20, v48\n"
      "v_exp_f32 v21, v49\n"
      "v_exp_f32 v22, v50\n"
      "v_exp_f32 v23, v51\n"
      "s_nop 7\n"
      "v_mfma_f32_16x16x32_bf16 a[32:35], a[224:227], v[84:87], 0\n"
      "v_exp_f32 v24, v52\n"
      "v_mfma_f32_16x16x32_bf16 v[132:135], v[152:155], v[84:87], 0\n"
      "v_exp_f32 v25, v53\n"
      "v_mfma_f32_16x16x32_bf16 a[36:39], a[228:231], v[84:87], 0\n"
      "v_cvt_pk_bf16_f32 v88, v20, v21\n"
      "v_cvt_pk_bf16_f32 v89, v22, v23\n"
      "v_mfma_f32_16x16x32_bf16 a[40:43], a[232:235], v[84:87], 0\n"
      "buffer_load_dwordx4 v1, s[20:23], s64 offen offset:0 nt lds\n"
      "v_mfma_f32_16x16x32_bf16 a[44:47], a[236:239], v[84:87], 0\n"
      "v_exp_f32 v156, v54\n"
      "s_nop 7\n"
      "v_mfma_f32_16x16x32_bf16 a[48:51], a[240:243], v[84:87], 0\n"
      "v_exp_f32 v157, v55\n"
      "v_mfma_f32_16x16x32_bf16 a[52:55], a[244:247], v[84:87], 0\n"
      "v_exp_f32 v20, v56\n"
      "v_mfma_f32_16x16x32_bf16 a[56:59], a[248:251], v[84:87], 0\n"
      "v_exp_f32 v21, v57\n"
      "v_mfma_f32_16x16x32_bf16 a[60:63], a[252:255], v[84:87], 0\n"
      "v_exp_f32 v22, v58\n"
      "s_nop 7\n"
      "v_mfma_f32_16x16x32_bf16 v[64:67], a[192:195], v[224:227], v[120:123]\n"
      "buffer_load_dwordx4 v2, s[20:23], s64 offen offset:1024 nt lds\n"
      "v_mfma_f32_16x16x32_bf16 v[72:75], a[192:195], v[240:243], v[124:127]\n"
      "v_exp_f32 v23, v59\n"
      "v_mfma_f32_16x16x32_bf16 v[68:71], a[196:199], v[224:227], v[120:123]\n"
      "v_cvt_pk_bf16_f32 v90, v24, v25\n"
      "v_cvt_pk_bf16_f32 v91, v156, v157\n"
      "v_mfma_f32_16x16x32_bf16 v[76:79], a[196:199], v[240:243], v[124:127]\n"
      "v_exp_f32 v24, v60\n"
      "v_mfma_f32_16x16x32_bf16 v[136:139], v[152:155], v[88:91], 0\n"
      "v_exp_f32 v25, v61\n"
      "s_nop 7\n"
      "v_mfma_f32_16x16x32_bf16 v[64:67], a[200:203], v[228:231], v[64:67]\n"
      "v_exp_f32 v156, v62\n"
      "v_mfma_f32_16x16x32_bf16 v[72:75], a[200:203], v[244:247], v[72:75]\n"
      "v_exp_f32 v157, v63\n"
      "v_mfma_f32_16x16x32_bf16 v[68:71], a[204:207], v[228:231], v[68:71]\n"
      "buffer_load_dwordx4 v3, s[20:23], s64 offen offset:2048 nt lds\n"
      "v_mfma_f32_16x16x32_bf16 v[76:79], a[204:207], v[244:247], v[76:79]\n"
      "v_cvt_pk_bf16_f32 v92, v20, v21\n"
      "v_cvt_pk_bf16_f32 v93, v22, v23\n"
      "s_nop 7\n"
      "v_mfma_f32_16x16x32_bf16 v[64:67], a[208:211], v[232:235], v[64:67]\n"
      "v_cvt_pk_bf16_f32 v94, v24, v25\n"
      "v_cvt_pk_bf16_f32 v95, v156, v157\n"
      "v_mfma_f32_16x16x32_bf16 v[72:75], a[208:211], v[248:251], v[72:75]\n"
      "s_nop 0\n"
      "v_mfma_f32_16x16x32_bf16 v[140:143], v[152:155], v[92:95], 0\n"
      "v_mfma_f32_16x16x32_bf16 v[68:71], a[212:215], v[232:235], v[68:71]\n"
      "buffer_load_dwordx4 v4, s[20:23], s64 offen offset:3072 nt lds\n"
      "v_mfma_f32_16x16x32_bf16 v[76:79], a[212:215], v[248:251], v[76:79]\n"
      "s_waitcnt vmcnt(16)\n"
      "s_nop 7\n"
      "v_mfma_f32_16x16x32_bf16 v[64:67], a[216:219], v[236:239], v[64:67]\n"
      "ds_read_b128 a[192:195], v5 offset:16384\n"
      "s_add_u32 m0, s40, 0x2000\n"
      "v_mfma_f32_16x16x32_bf16 v[72:75], a[216:219], v[252:255], v[72:75]\n"
      "ds_read_b128 a[196:199], v6 offset:16384\n"
      "ds_read_b128 a[200:203], v5 offset:16896\n"
      "v_mfma_f32_16x16x32_bf16 v[68:71], a[220:223], v[236:239], v[68:71]\n"
      "ds_read_b128 a[204:207], v6 offset:16896\n"
      "ds_read_b128 a[208:211], v5 offset:17408\n"
      "v_mfma_f32_16x16x32_bf16 v[76:79], a[220:223], v[252:255], v[76:79]\n"
      "ds_read_b128 a[212:215], v6 offset:17408\n"
      "ds_read_b128 a[216:219], v5 offset:17920\n"
      "s_nop 7\n"
      "v_mfma_f32_16x16x32_bf16 a[64:67], a[224:227], v[88:91], 0\n"
      "ds_read_b128 a[220:223], v6 offset:17920\n"
      "v_mfma_f32_16x16x32_bf16 a[68:71], a[228:231], v[88:91], 0\n"
      "v_exp_f32 v20, v64\n"
      "v_mfma_f32_16x16x32_bf16 a[72:75], a[232:235], v[88:91], 0\n"
      "buffer_load_dwordx4 v1, s[24:27], s63 offen offset:0 nt lds\n"
      "v_mfma_f32_16x16x32_bf16 a[76:79], a[236:239], v[88:91], 0\n"
      "v_exp_f32 v21, v65\n"
      "s_nop 7\n"
      "v_mfma_f32_16x16x32_bf16 a[80:83], a[240:243], v[88:91], 0\n"
      "v_exp_f32 v22, v66\n"
      "v_mfma_f32_16x16x32_bf16 a[84:87], a[244:247], v[88:91], 0\n"
      "v_exp_f32 v23, v67\n"
      "v_mfma_f32_16x16x32_bf16 a[88:91], a[248:251], v[88:91], 0\n"
      "v_exp_f32 v24, v68\n"
      "v_mfma_f32_16x16x32_bf16 a[92:95], a[252:255], v[88:91], 0\n"
      "v_exp_f32 v25, v69\n"
      "s_nop 7\n"
      "v_mfma_f32_16x16x32_bf16 a[96:99], a[224:227], v[92:95], 0\n"
      "buffer_load_dwordx4 v2, s[24:27], s63 offen offset:1024 nt lds\n"
      "v_mfma_f32_16x16x32_bf16 a[100:103], a[228:231], v[92:95], 0\n"
      "v_cvt_pk_bf16_f32 v96, v20, v21\n"
      "v_cvt_pk_bf16_f32 v97, v22, v23\n"
      "v_mfma_f32_16x16x32_bf16 a[104:107], a[232:235], v[92:95], 0\n"
      "v_exp_f32 v156, v70\n"
      "v_mfma_f32_16x16x32_bf16 a[108:111], a[236:239], v[92:95], 0\n"
      "v_exp_f32 v157, v71\n"
      "s_nop 7\n"
      "v_mfma_f32_16x16x32_bf16 a[112:115], a[240:243], v[92:95], 0\n"
      "v_exp_f32 v20, v72\n"
      "v_mfma_f32_16x16x32_bf16 a[116:119], a[244:247], v[92:95], 0\n"
      "v_exp_f32 v21, v73\n"
      "v_mfma_f32_16x16x32_bf16 a[120:123], a[248:251], v[92:95], 0\n"
      "buffer_load_dwordx4 v3, s[24:27], s63 offen offset:2048 nt lds\n"
      "v_mfma_f32_16x16x32_bf16 a[124:127], a[252:255], v[92:95], 0\n"
      "s_waitcnt lgkmcnt(0)\n"
      "s_nop 7\n"
      "v_mfma_f32_16x16x32_bf16 v[32:35], a[192:195], v[160:163], v[104:107]\n"
      "v_exp_f32 v22, v74\n"
      "v_mfma_f32_16x16x32_bf16 v[40:43], a[192:195], v[176:179], v[108:111]\n"
      "v_exp_f32 v23, v75\n"
      "v_mfma_f32_16x16x32_bf16 v[36:39], a[196:199], v[160:163], v[104:107]\n"
      "v_cvt_pk_bf16_f32 v98, v24, v25\n"
      "v_cvt_pk_bf16_f32 v99, v156, v157\n"
      "v_mfma_f32_16x16x32_bf16 v[44:47], a[196:199], v[176:179], v[108:111]\n"
      "v_exp_f32 v24, v76\n"
      "v_mfma_f32_16x16x32_bf16 v[144:147], v[152:155], v[96:99], 0\n"
      "v_exp_f32 v25, v77\n"
      "s_nop 7\n"
      "v_mfma_f32_16x16x32_bf16 v[32:35], a[200:203], v[164:167], v[32:35]\n"
      "buffer_load_dwordx4 v4, s[24:27], s63 offen offset:3072 nt lds\n"
      "v_mfma_f32_16x16x32_bf16 v[40:43], a[200:203], v[180:183], v[40:43]\n"
      "s_add_u32 m0, s40, 0x3000\n"
      "v_cvt_pk_bf16_f32 v100, v20, v21\n"
      "v_mfma_f32_16x16x32_bf16 v[36:39], a[204:207], v[164:167], v[36:39]\n"
      "v_exp_f32 v156, v78\n"
      "v_mfma_f32_16x16x32_bf16 v[44:47], a[204:207], v[180:183], v[44:47]\n"
      "v_exp_f32 v157, v79\n"
      "s_nop 7\n"
      "v_mfma_f32_16x16x32_bf16 v[32:35], a[208:211], v[168:171], v[32:35]\n"
      "v_cvt_pk_bf16_f32 v101, v22, v23\n"
      "v_cvt_pk_bf16_f32 v102, v24, v25\n"
      "v_mfma_f32_16x16x32_bf16 v[40:43], a[208:211], v[184:187], v[40:43]\n"
      "v_cvt_pk_bf16_f32 v103, v156, v157\n"
      "v_mfma_f32_16x16x32_bf16 v[36:39], a[212:215], v[168:171], v[36:39]\n"
      "buffer_load_dwordx4 v1, s[24:27], s64 offen offset:0 nt lds\n"
      "v_mfma_f32_16x16x32_bf16 v[148:151], v[152:155], v[100:103], 0\n"
      "v_mfma_f32_16x16x32_bf16 v[44:47], a[212:215], v[184:187], v[44:47]\n"
      "s_nop 7\n"
      "v_mfma_f32_16x16x32_bf16 v[32:35], a[216:219], v[172:175], v[32:35]\n"
      "v_mfma_f32_16x16x32_bf16 v[40:43], a[216:219], v[188:191], v[40:43]\n"
      "v_mfma_f32_16x16x32_bf16 v[36:39], a[220:223], v[172:175], v[36:39]\n"
      "buffer_load_dwordx4 v2, s[24:27], s64 offen offset:1024 nt lds\n"
      "v_mfma_f32_16x16x32_bf16 v[44:47], a[220:223], v[188:191], v[44:47]\n"
      "s_nop 7\n"
      "v_mfma_f32_16x16x32_bf16 a[128:131], a[224:227], v[96:99], 0\n"
      "v_mfma_f32_16x16x32_bf16 a[132:135], a[228:231], v[96:99], 0\n"
      "v_exp_f32 v20, v32\n"
      "v_mfma_f32_16x16x32_bf16 a[136:139], a[232:235], v[96:99], 0\n"
      "v_exp_f32 v21, v33\n"
      "v_mfma_f32_16x16x32_bf16 a[140:143], a[236:239], v[96:99], 0\n"
      "v_exp_f32 v22, v34\n"
      "s_nop 7\n"
      "v_mfma_f32_16x16x32_bf16 a[144:147], a[240:243], v[96:99], 0\n"
      "v_exp_f32 v23, v35\n"
      "v_mfma_f32_16x16x32_bf16 a[148:151], a[244:247], v[96:99], 0\n"
      "v_exp_f32 v24, v36\n"
      "v_mfma_f32_16x16x32_bf16 a[152:155], a[248:251], v[96:99], 0\n"
      "buffer_load_dwordx4 v3, s[24:27], s64 offen offset:2048 nt lds\n"
      "v_mfma_f32_16x16x32_bf16 a[156:159], a[252:255], v[96:99], 0\n"
      "v_exp_f32 v25, v37\n"
      "s_nop 7\n"
      "v_mfma_f32_16x16x32_bf16 a[160:163], a[224:227], v[100:103], 0\n"
      "v_exp_f32 v156, v38\n"
      "v_mfma_f32_16x16x32_bf16 a[164:167], a[228:231], v[100:103], 0\n"
      "v_cvt_pk_bf16_f32 v80, v20, v21\n"
      "s_waitcnt vmcnt(15)\n"
      "v_mfma_f32_16x16x32_bf16 a[168:171], a[232:235], v[100:103], 0\n"
      "v_cvt_pk_bf16_f32 v81, v22, v23\n"
      "v_cvt_pk_bf16_f32 v82, v24, v25\n"
      "v_mfma_f32_16x16x32_bf16 a[172:175], a[236:239], v[100:103], 0\n"
      "v_exp_f32 v157, v39\n"
      "s_nop 7\n"
      "v_mfma_f32_16x16x32_bf16 a[176:179], a[240:243], v[100:103], 0\n"
      "buffer_load_dwordx4 v4, s[24:27], s64 offen offset:3072 nt lds\n"
      "v_mfma_f32_16x16x32_bf16 a[180:183], a[244:247], v[100:103], 0\n"
      "s_add_u32 m0, s40, 0x4000\n"
      "s_add_u32 s63, s63, 0x8000\n"
      "v_mfma_f32_16x16x32_bf16 a[184:187], a[248:251], v[100:103], 0\n"
      "v_exp_f32 v20, v40\n"
      "v_mfma_f32_16x16x32_bf16 a[188:191], a[252:255], v[100:103], 0\n"
      "v_exp_f32 v21, v41\n"
      "s_nop 7\n"
      "v_mfma_f32_16x16x32_bf16 v[48:51], a[192:195], v[192:195], v[112:115]\n"
      "ds_read_b64_tr_b16 a[224:225], v7 offset:16384\n"
      "v_cvt_pk_bf16_f32 v83, v156, v157\n"
      "v_mfma_f32_16x16x32_bf16 v[56:59], a[192:195], v[208:211], v[116:119]\n"
      "v_exp_f32 v22, v42\n"
      "v_mfma_f32_16x16x32_bf16 v[128:131], v[152:155], v[80:83], v[128:131]\n"
      "ds_read_b64_tr_b16 a[226:227], v8 offset:16384\n"
      "v_cvt_pk_bf16_f32 v84, v20, v21\n"
      "v_mfma_f32_16x16x32_bf16 v[52:55], a[196:199], v[192:195], v[112:115]\n"
      "buffer_load_dwordx4 v1, s[20:23], s63 offen offset:0 nt lds\n"
      "v_mfma_f32_16x16x32_bf16 v[60:63], a[196:199], v[208:211], v[116:119]\n"
      "v_exp_f32 v23, v43\n"
      "s_nop 7\n"
      "v_mfma_f32_16x16x32_bf16 v[48:51], a[200:203], v[196:199], v[48:51]\n"
      "ds_read_b64_tr_b16 a[228:229], v9 offset:16384\n"
      "ds_read_b64_tr_b16 a[230:231], v10 offset:16384\n"
      "v_mfma_f32_16x16x32_bf16 v[56:59], a[200:203], v[212:215], v[56:59]\n"
      "ds_read_b64_tr_b16 a[232:233], v7 offset:16896\n"
      "ds_read_b64_tr_b16 a[234:235], v8 offset:16896\n"
      "v_mfma_f32_16x16x32_bf16 v[52:55], a[204:207], v[196:199], v[52:55]\n"
      "ds_read_b64_tr_b16 a[236:237], v9 offset:16896\n"
      "ds_read_b64_tr_b16 a[238:239], v10 offset:16896\n"
      "v_mfma_f32_16x16x32_bf16 v[60:63], a[204:207], v[212:215], v[60:63]\n"
      "v_exp_f32 v24, v44\n"
      "s_nop 7\n"
      "v_mfma_f32_16x16x32_bf16 v[48:51], a[208:211], v[200:203], v[48:51]\n"
      "buffer_load_dwordx4 v2, s[20:23], s63 offen offset:1024 nt lds\n"
      "v_mfma_f32_16x16x32_bf16 v[56:59], a[208:211], v[216:219], v[56:59]\n"
      "ds_read_b64_tr_b16 a[240:241], v7 offset:17408\n"
      "ds_read_b64_tr_b16 a[242:243], v8 offset:17408\n"
      "v_mfma_f32_16x16x32_bf16 v[52:55], a[212:215], v[200:203], v[52:55]\n"
      "ds_read_b64_tr_b16 a[244:245], v9 offset:17408\n"
      "ds_read_b64_tr_b16 a[246:247], v10 offset:17408\n"
      "v_mfma_f32_16x16x32_bf16 v[60:63], a[212:215], v[216:219], v[60:63]\n"
      "v_exp_f32 v25, v45\n"
      "s_nop 7\n"
      "v_mfma_f32_16x16x32_bf16 v[48:51], a[216:219], v[204:207], v[48:51]\n"
      "ds_read_b64_tr_b16 a[248:249], v7 offset:17920\n"
      "ds_read_b64_tr_b16 a[250:251], v8 offset:17920\n"
      "v_mfma_f32_16x16x32_bf16 v[56:59], a[216:219], v[220:223], v[56:59]\n"
      "ds_read_b64_tr_b16 a[252:253], v9 offset:17920\n"
      "v_cvt_pk_bf16_f32 v85, v22, v23\n"
      "v_mfma_f32_16x16x32_bf16 v[52:55], a[220:223], v[204:207], v[52:55]\n"
      "buffer_load_dwordx4 v3, s[20:23], s63 offen offset:2048 nt lds\n"
      "v_mfma_f32_16x16x32_bf16 v[60:63], a[220:223], v[220:223], v[60:63]\n"
      "ds_read_b64_tr_b16 a[254:255], v10 offset:17920\n"
      "s_waitcnt lgkmcnt(8)\n"
      "s_nop 7\n"
      "v_mfma_f32_16x16x32_bf16 a[0:3], a[224:227], v[80:83], a[0:3]\n"
      "v_exp_f32 v156, v46\n"
      "v_mfma_f32_16x16x32_bf16 a[4:7], a[228:231], v[80:83], a[4:7]\n"
      "v_exp_f32 v157, v47\n"
      "v_mfma_f32_16x16x32_bf16 a[8:11], a[232:235], v[80:83], a[8:11]\n"
      "s_waitcnt lgkmcnt(0)\n"
      "v_mfma_f32_16x16x32_bf16 a[12:15], a[236:239], v[80:83], a[12:15]\n"
      "v_cvt_pk_bf16_f32 v86, v24, v25\n"
      "v_cvt_pk_bf16_f32 v87, v156, v157\n"
      "v_exp_f32 v20, v48\n"
      "v_exp_f32 v21, v49\n"
      "s_nop 7\n"
      "v_mfma_f32_16x16x32_bf16 a[16:19], a[240:243], v[80:83], a[16:19]\n"
      "buffer_load_dwordx4 v4, s[20:23], s63 offen offset:3072 nt lds\n"
      "v_mfma_f32_16x16x32_bf16 v[132:135], v[152:155], v[84:87], v[132:135]\n"
      "v_exp_f32 v22, v50\n"
      "v_mfma_f32_16x16x32_bf16 a[20:23], a[244:247], v[80:83], a[20:23]\n"
      "s_add_u32 m0, s40, 0x5000\n"
      "s_add_u32 s64, s64, 0x8000\n"
      "v_mfma_f32_16x16x32_bf16 a[24:27], a[248:251], v[80:83], a[24:27]\n"
      "v_exp_f32 v23, v51\n"
      "v_mfma_f32_16x16x32_bf16 a[28:31], a[252:255], v[80:83], a[28:31]\n"
      "v_exp_f32 v24, v52\n"
      "s_nop 7\n"
      "v_mfma_f32_16x16x32_bf16 a[32:35], a[224:227], v[84:87], a[32:35]\n"
      "v_exp_f32 v25, v53\n"
      "v_mfma_f32_16x16x32_bf16 a[36:39], a[228:231], v[84:87], a[36:39]\n"
      "v_cvt_pk_bf16_f32 v88, v20, v21\n"
      "v_cvt_pk_bf16_f32 v89, v22, v23\n"
      "v_mfma_f32_16x16x32_bf16 a[40:43], a[232:235], v[84:87], a[40:43]\n"
      "buffer_load_dwordx4 v1, s[20:23], s64 offen offset:0 nt lds\n"
      "v_mfma_f32_16x16x32_bf16 a[44:47], a[236:239], v[84:87], a[44:47]\n"
      "v_exp_f32 v156, v54\n"
      "s_nop 7\n"
      "v_mfma_f32_16x16x32_bf16 a[48:51], a[240:243], v[84:87], a[48:51]\n"
      "v_exp_f32 v157, v55\n"
      "v_mfma_f32_16x16x32_bf16 a[52:55], a[244:247], v[84:87], a[52:55]\n"
      "v_exp_f32 v20, v56\n"
      "v_mfma_f32_16x16x32_bf16 a[56:59], a[248:251], v[84:87], a[56:59]\n"
      "v_exp_f32 v21, v57\n"
      "v_mfma_f32_16x16x32_bf16 a[60:63], a[252:255], v[84:87], a[60:63]\n"
      "v_exp_f32 v22, v58\n"
      "s_nop 7\n"
      "v_mfma_f32_16x16x32_bf16 v[64:67], a[192:195], v[224:227], v[120:123]\n"
      "buffer_load_dwordx4 v2, s[20:23], s64 offen offset:1024 nt lds\n"
      "v_mfma_f32_16x16x32_bf16 v[72:75], a[192:195], v[240:243], v[124:127]\n"
      "v_exp_f32 v23, v59\n"
      "v_mfma_f32_16x16x32_bf16 v[68:71], a[196:199], v[224:227], v[120:123]\n"
      "v_cvt_pk_bf16_f32 v90, v24, v25\n"
      "v_cvt_pk_bf16_f32 v91, v156, v157\n"
      "v_mfma_f32_16x16x32_bf16 v[76:79], a[196:199], v[240:243], v[124:127]\n"
      "v_exp_f32 v24, v60\n"
      "v_mfma_f32_16x16x32_bf16 v[136:139], v[152:155], v[88:91], v[136:139]\n"
      "v_exp_f32 v25, v61\n"
      "s_nop 7\n"
      "v_mfma_f32_16x16x32_bf16 v[64:67], a[200:203], v[228:231], v[64:67]\n"
      "v_exp_f32 v156, v62\n"
      "v_mfma_f32_16x16x32_bf16 v[72:75], a[200:203], v[244:247], v[72:75]\n"
      "v_exp_f32 v157, v63\n"
      "v_mfma_f32_16x16x32_bf16 v[68:71], a[204:207], v[228:231], v[68:71]\n"
      "buffer_load_dwordx4 v3, s[20:23], s64 offen offset:2048 nt lds\n"
      "v_mfma_f32_16x16x32_bf16 v[76:79], a[204:207], v[244:247], v[76:79]\n"
      "v_cvt_pk_bf16_f32 v92, v20, v21\n"
      "v_cvt_pk_bf16_f32 v93, v22, v23\n"
      "s_nop 7\n"
      "v_mfma_f32_16x16x32_bf16 v[64:67], a[208:211], v[232:235], v[64:67]\n"
      "v_cvt_pk_bf16_f32 v94, v24, v25\n"
      "v_cvt_pk_bf16_f32 v95, v156, v157\n"
      "v_mfma_f32_16x16x32_bf16 v[72:75], a[208:211], v[248:251], v[72:75]\n"
      "s_nop 0\n"
      "v_mfma_f32_16x16x32_bf16 v[140:143], v[152:155], v[92:95], v[140:143]\n"
      "v_mfma_f32_16x16x32_bf16 v[68:71], a[212:215], v[232:235], v[68:71]\n"
      "buffer_load_dwordx4 v4, s[20:23], s64 offen offset:3072 nt lds\n"
      "v_mfma_f32_16x16x32_bf16 v[76:79], a[212:215], v[248:251], v[76:79]\n"
      "s_waitcnt vmcnt(16)\n"
      "s_nop 7\n"
      "v_mfma_f32_16x16x32_bf16 v[64:67], a[216:219], v[236:239], v[64:67]\n"
      "ds_read_b128 a[192:195], v5 offset:0\n"
      "s_add_u32 m0, s40, 0x6000\n"
      "v_mfma_f32_16x16x32_bf16 v[72:75], a[216:219], v[252:255], v[72:75]\n"
      "ds_read_b128 a[196:199], v6 offset:0\n"
      "ds_read_b128 a[200:203], v5 offset:512\n"
      "v_mfma_f32_16x16x32_bf16 v[68:71], a[220:223], v[236:239], v[68:71]\n"
      "ds_read_b128 a[204:207], v6 offset:512\n"
      "ds_read_b128 a[208:211], v5 offset:1024\n"
      "v_mfma_f32_16x16x32_bf16 v[76:79], a[220:223], v[252:255], v[76:79]\n"
      "ds_read_b128 a[212:215], v6 offset:1024\n"
      "ds_read_b128 a[216:219], v5 offset:1536\n"
      "s_nop 7\n"
      "v_mfma_f32_16x16x32_bf16 a[64:67], a[224:227], v[88:91], a[64:67]\n"
      "ds_read_b128 a[220:223], v6 offset:1536\n"
      "v_mfma_f32_16x16x32_bf16 a[68:71], a[228:231], v[88:91], a[68:71]\n"
      "v_exp_f32 v20, v64\n"
      "v_mfma_f32_16x16x32_bf16 a[72:75], a[232:235], v[88:91], a[72:75]\n"
      "buffer_load_dwordx4 v1, s[24:27], s63 offen offset:0 nt lds\n"
      "v_mfma_f32_16x16x32_bf16 a[76:79], a[236:239], v[88:91], a[76:79]\n"
      "v_exp_f32 v21, v65\n"
      "s_nop 7\n"
      "v_mfma_f32_16x16x32_bf16 a[80:83], a[240:243], v[88:91], a[80:83]\n"
      "v_exp_f32 v22, v66\n"
      "v_mfma_f32_16x16x32_bf16 a[84:87], a[244:247], v[88:91], a[84:87]\n"
      "v_exp_f32 v23, v67\n"
      "v_mfma_f32_16x16x32_bf16 a[88:91], a[248:251], v[88:91], a[88:91]\n"
      "v_exp_f32 v24, v68\n"
      "v_mfma_f32_16x16x32_bf16 a[92:95], a[252:255], v[88:91], a[92:95]\n"
      "v_exp_f32 v25, v69\n"
      "s_nop 7\n"
      "v_mfma_f32_16x16x32_bf16 a[96:99], a[224:227], v[92:95], a[96:99]\n"
      "buffer_load_dwordx4 v2, s[24:27], s63 offen offset:1024 nt lds\n"
      "v_mfma_f32_16x16x32_bf16 a[100:103], a[228:231], v[92:95], a[100:103]\n"
      "v_cvt_pk_bf16_f32 v96, v20, v21\n"
      "v_cvt_pk_bf16_f32 v97, v22, v23\n"
      "v_mfma_f32_16x16x32_bf16 a[104:107], a[232:235], v[92:95], a[104:107]\n"
      "v_exp_f32 v156, v70\n"
      "v_mfma_f32_16x16x32_bf16 a[108:111], a[236:239], v[92:95], a[108:111]\n"
      "v_exp_f32 v157, v71\n"
      "s_nop 7\n"
      "v_mfma_f32_16x16x32_bf16 a[112:115], a[240:243], v[92:95], a[112:115]\n"
      "v_exp_f32 v20, v72\n"
      "v_mfma_f32_16x16x32_bf16 a[116:119], a[244:247], v[92:95], a[116:119]\n"
      "v_exp_f32 v21, v73\n"
      "v_mfma_f32_16x16x32_bf16 a[120:123], a[248:251], v[92:95], a[120:123]\n"
      "buffer_load_dwordx4 v3, s[24:27], s63 offen offset:2048 nt lds\n"
      "v_mfma_f32_16x16x32_bf16 a[124:127], a[252:255], v[92:95], a[124:127]\n"
      "s_waitcnt lgkmcnt(0)\n"
      "s_nop 7\n"
      "v_mfma_f32_16x16x32_bf16 v[32:35], a[192:195], v[160:163], v[104:107]\n"
      "v_exp_f32 v22, v74\n"
      "v_mfma_f32_16x16x32_bf16 v[40:43], a[192:195], v[176:179], v[108:111]\n"
      "v_exp_f32 v23, v75\n"
      "v_mfma_f32_16x16x32_bf16 v[36:39], a[196:199], v[160:163], v[104:107]\n"
      "v_cvt_pk_bf16_f32 v98, v24, v25\n"
      "v_cvt_pk_bf16_f32 v99, v156, v157\n"
      "v_mfma_f32_16x16x32_bf16 v[44:47], a[196:199], v[176:179], v[108:111]\n"
      "v_exp_f32 v24, v76\n"
      "v_mfma_f32_16x16x32_bf16 v[144:147], v[152:155], v[96:99], v[144:147]\n"
      "v_exp_f32 v25, v77\n"
      "s_nop 7\n"
      "v_mfma_f32_16x16x32_bf16 v[32:35], a[200:203], v[164:167], v[32:35]\n"
      "buffer_load_dwordx4 v4, s[24:27], s63 offen offset:3072 nt lds\n"
      "v_mfma_f32_16x16x32_bf16 v[40:43], a[200:203], v[180:183], v[40:43]\n"
      "s_add_u32 m0, s40, 0x7000\n"
      "v_cvt_pk_bf16_f32 v100, v20, v21\n"
      "v_mfma_f32_16x16x32_bf16 v[36:39], a[204:207], v[164:167], v[36:39]\n"
      "v_exp_f32 v156, v78\n"
      "v_mfma_f32_16x16x32_bf16 v[44:47], a[204:207], v[180:183], v[44:47]\n"
      "v_exp_f32 v157, v79\n"
      "s_nop 7\n"
      "v_mfma_f32_16x16x32_bf16 v[32:35], a[208:211], v[168:171], v[32:35]\n"
      "v_cvt_pk_bf16_f32 v101, v22, v23\n"
      "v_cvt_pk_bf16_f32 v102, v24, v25\n"
      "v_mfma_f32_16x16x32_bf16 v[40:43], a[208:211], v[184:187], v[40:43]\n"
      "v_cvt_pk_bf16_f32 v103, v156, v157\n"
      "v_mfma_f32_16x16x32_bf16 v[36:39], a[212:215], v[168:171], v[36:39]\n"
      "buffer_load_dwordx4 v1, s[24:27], s64 offen offset:0 nt lds\n"
      "v_mfma_f32_16x16x32_bf16 v[148:151], v[152:155], v[100:103], v[148:151]\n"
      "v_mfma_f32_16x16x32_bf16 v[44:47], a[212:215], v[184:187], v[44:47]\n"
      "s_nop 7\n"
      "v_mfma_f32_16x16x32_bf16 v[32:35], a[216:219], v[172:175], v[32:35]\n"
      "v_mfma_f32_16x16x32_bf16 v[40:43], a[216:219], v[188:191], v[40:43]\n"
      "v_mfma_f32_16x16x32_bf16 v[36:39], a[220:223], v[172:175], v[36:39]\n"
      "buffer_load_dwordx4 v2, s[24:27], s64 offen offset:1024 nt lds\n"
      "v_mfma_f32_16x16x32_bf16 v[44:47], a[220:223], v[188:191], v[44:47]\n"
      "s_nop 7\n"
      "v_mfma_f32_16x16x32_bf16 a[128:131], a[224:227], v[96:99], a[128:131]\n"
      "v_mfma_f32_16x16x32_bf16 a[132:135], a[228:231], v[96:99], a[132:135]\n"
      "v_exp_f32 v20, v32\n"
      "v_mfma_f32_16x16x32_bf16 a[136:139], a[232:235], v[96:99], a[136:139]\n"
      "v_exp_f32 v21, v33\n"
      "v_mfma_f32_16x16x32_bf16 a[140:143], a[236:239], v[96:99], a[140:143]\n"
      "v_exp_f32 v22, v34\n"
      "s_nop 7\n"
      "v_mfma_f32_16x16x32_bf16 a[144:147], a[240:243], v[96:99], a[144:147]\n"
      "v_exp_f32 v23, v35\n"
      "v_mfma_f32_16x16x32_bf16 a[148:151], a[244:247], v[96:99], a[148:151]\n"
      "v_exp_f32 v24, v36\n"
      "v_mfma_f32_16x16x32_bf16 a[152:155], a[248:251], v[96:99], a[152:155]\n"
      "buffer_load_dwordx4 v3, s[24:27], s64 offen offset:2048 nt lds\n"
      "v_mfma_f32_16x16x32_bf16 a[156:159], a[252:255], v[96:99], a[156:159]\n"
      "v_exp_f32 v25, v37\n"
      "s_nop 7\n"
      "v_mfma_f32_16x16x32_bf16 a[160:163], a[224:227], v[100:103], a[160:163]\n"
      "v_exp_f32 v156, v38\n"
      "v_mfma_f32_16x16x32_bf16 a[164:167], a[228:231], v[100:103], a[164:167]\n"
      "v_cvt_pk_bf16_f32 v80, v20, v21\n"
      "s_waitcnt vmcnt(15)\n"
      "v_mfma_f32_16x16x32_bf16 a[168:171], a[232:235], v[100:103], a[168:171]\n"
      "v_cvt_pk_bf16_f32 v81, v22, v23\n"
      "v_cvt_pk_bf16_f32 v82, v24, v25\n"
      "v_mfma_f32_16x16x32_bf16 a[172:175], a[236:239], v[100:103], a[172:175]\n"
      "v_exp_f32 v157, v39\n"
      "s_nop 7\n"
      "v_mfma_f32_16x16x32_bf16 a[176:179], a[240:243], v[100:103], a[176:179]\n"
      "buffer_load_dwordx4 v4, s[24:27], s64 offen offset:3072 nt lds\n"
      "v_mfma_f32_16x16x32_bf16 a[180:183], a[244:247], v[100:103], a[180:183]\n"
      "s_mov_b32 m0, s40\n"
      "s_add_u32 s63, s63, 0x8000\n"
      "v_mfma_f32_16x16x32_bf16 a[184:187], a[248:251], v[100:103], a[184:187]\n"
      "v_exp_f32 v20, v40\n"
      "v_mfma_f32_16x16x32_bf16 a[188:191], a[252:255], v[100:103], a[188:191]\n"
      "v_exp_f32 v21, v41\n"
      "s_nop 7\n"
      "v_mfma_f32_16x16x32_bf16 v[48:51], a[192:195], v[192:195], v[112:115]\n"
      "ds_read_b64_tr_b16 a[224:225], v7 offset:0\n"
      "v_cvt_pk_bf16_f32 v83, v156, v157\n"
      "v_mfma_f32_16x16x32_bf16 v[56:59], a[192:195], v[208:211], v[116:119]\n"
      "v_exp_f32 v22, v42\n"
      "v_mfma_f32_16x16x32_bf16 v[128:131], v[152:155], v[80:83], v[128:131]\n"
      "ds_read_b64_tr_b16 a[226:227], v8 offset:0\n"
      "v_cvt_pk_bf16_f32 v84, v20, v21\n"
      "v_mfma_f32_16x16x32_bf16 v[52:55], a[196:199], v[192:195], v[112:115]\n"
      "v_exp_f32 v23, v43\n"
      "v_mfma_f32_16x16x32_bf16 v[60:63], a[196:199], v[208:211], v[116:119]\n"
      "ds_read_b64_tr_b16 a[228:229], v9 offset:0\n"
      "ds_read_b64_tr_b16 a[230:231], v10 offset:0\n"
      "s_nop 7\n"
      "v_mfma_f32_16x16x32_bf16 v[48:51], a[200:203], v[196:199], v[48:51]\n"
      "buffer_load_dwordx4 v1, s[20:23], s63 offen offset:0 nt lds\n"
      "v_mfma_f32_16x16x32_bf16 v[56:59], a[200:203], v[212:215], v[56:59]\n"
      "ds_read_b64_tr_b16 a[232:233], v7 offset:512\n"
      "ds_read_b64_tr_b16 a[234:235], v8 offset:512\n"
      "v_mfma_f32_16x16x32_bf16 v[52:55], a[204:207], v[196:199], v[52:55]\n"
      "ds_read_b64_tr_b16 a[236:237], v9 offset:512\n"
      "ds_read_b64_tr_b16 a[238:239], v10 offset:512\n"
      "v_mfma_f32_16x16x32_bf16 v[60:63], a[204:207], v[212:215], v[60:63]\n"
      "v_exp_f32 v24, v44\n"
      "s_nop 7\n"
      "v_mfma_f32_16x16x32_bf16 v[48:51], a[208:211], v[200:203], v[48:51]\n"
      "ds_read_b64_tr_b16 a[240:241], v7 offset:1024\n"
      "ds_read_b64_tr_b16 a[242:243], v8 offset:1024\n"
      "v_mfma_f32_16x16x32_bf16 v[56:59], a[208:211], v[216:219], v[56:59]\n"
      "ds_read_b64_tr_b16 a[244:245], v9 offset:1024\n"
      "ds_read_b64_tr_b16 a[246:247], v10 offset:1024\n"
      "v_mfma_f32_16x16x32_bf16 v[52:55], a[212:215], v[200:203], v[52:55]\n"
      "buffer_load_dwordx4 v2, s[20:23], s63 offen offset:1024 nt lds\n"
      "v_mfma_f32_16x16x32_bf16 v[60:63], a[212:215], v[216:219], v[60:63]\n"
      "v_exp_f32 v25, v45\n"
      "s_nop 7\n"
      "v_mfma_f32_16x16x32_bf16 v[48:51], a[216:219], v[204:207], v[48:51]\n"
      "ds_read_b64_tr_b16 a[248:249], v7 offset:1536\n"
      "ds_read_b64_tr_b16 a[250:251], v8 offset:1536\n"
      "v_mfma_f32_16x16x32_bf16 v[56:59], a[216:219], v[220:223], v[56:59]\n"
      "ds_read_b64_tr_b16 a[252:253], v9 offset:1536\n"
      "v_cvt_pk_bf16_f32 v85, v22, v23\n"
      "v_mfma_f32_16x16x32_bf16 v[52:55], a[220:223], v[204:207], v[52:55]\n"
      "ds_read_b64_tr_b16 a[254:255], v10 offset:1536\n"
      "s_waitcnt lgkmcnt(8)\n"
      "v_mfma_f32_16x16x32_bf16 v[60:63], a[220:223], v[220:223], v[60:63]\n"
      "v_exp_f32 v156, v46\n"
      "s_nop 7\n"
      "v_mfma_f32_16x16x32_bf16 a[0:3], a[224:227], v[80:83], a[0:3]\n"
      "buffer_load_dwordx4 v3, s[20:23], s63 offen offset:2048 nt lds\n"
      "v_mfma_f32_16x16x32_bf16 a[4:7], a[228:231], v[80:83], a[4:7]\n"
      "v_exp_f32 v157, v47\n"
      "v_mfma_f32_16x16x32_bf16 a[8:11], a[232:235], v[80:83], a[8:11]\n"
      "s_waitcnt lgkmcnt(0)\n"
      "s_cmp_lg_u32 s43, 0\n"
      "s_cbranch_scc1 NONCED_%=\n"
      "s_waitcnt vmcnt(47)\n"
      "v_add_u32 v159, 1, v159\n"
      "v_add_u32 v26, 1, v159\n"
      "v_cmp_eq_u32 vcc, v159, v19\n"
      "s_nop 1\n"
      "v_cndmask_b32 v159, v159, v26, vcc\n"
      "s_mov_b64 exec, 1\n"
      "buffer_store_dword v159, off, s[16:19], s48 sc1\n"
      "s_mov_b64 exec, -1\n"
      "v_readfirstlane_b32 s44, v159\n"
      "NONCED_%=:\n"
      "s_barrier\n"
      "v_mfma_f32_16x16x32_bf16 a[12:15], a[236:239], v[80:83], a[12:15]\n"
      "v_cvt_pk_bf16_f32 v86, v24, v25\n"
      "v_cvt_pk_bf16_f32 v87, v156, v157\n"
      "v_exp_f32 v20, v48\n"
      "v_exp_f32 v21, v49\n"
      "s_nop 7\n"
      "v_mfma_f32_16x16x32_bf16 a[16:19], a[240:243], v[80:83], a[16:19]\n"
      "v_exp_f32 v22, v50\n"
      "v_mfma_f32_16x16x32_bf16 v[132:135], v[152:155], v[84:87], v[132:135]\n"
      "v_exp_f32 v23, v51\n"
      "v_mfma_f32_16x16x32_bf16 a[20:23], a[244:247], v[80:83], a[20:23]\n"
      "v_exp_f32 v24, v52\n"
      "v_mfma_f32_16x16x32_bf16 a[24:27], a[248:251], v[80:83], a[24:27]\n"
      "buffer_load_dwordx4 v4, s[20:23], s63 offen offset:3072 nt lds\n"
      "v_mfma_f32_16x16x32_bf16 a[28:31], a[252:255], v[80:83], a[28:31]\n"
      "s_add_u32 m0, s40, 0x1000\n"
      "s_add_u32 s64, s64, 0x8000\n"
      "s_nop 7\n"
      "v_mfma_f32_16x16x32_bf16 a[32:35], a[224:227], v[84:87], a[32:35]\n"
      "v_exp_f32 v25, v53\n"
      "v_mfma_f32_16x16x32_bf16 a[36:39], a[228:231], v[84:87], a[36:39]\n"
      "v_cvt_pk_bf16_f32 v88, v20, v21\n"
      "v_cvt_pk_bf16_f32 v89, v22, v23\n"
      "v_mfma_f32_16x16x32_bf16 a[40:43], a[232:235], v[84:87], a[40:43]\n"
      "v_exp_f32 v156, v54\n"
      "v_mfma_f32_16x16x32_bf16 a[44:47], a[236:239], v[84:87], a[44:47]\n"
      "v_exp_f32 v157, v55\n"
      "s_nop 7\n"
      "v_mfma_f32_16x16x32_bf16 a[48:51], a[240:243], v[84:87], a[48:51]\n"
      "v_exp_f32 v20, v56\n"
      "v_mfma_f32_16x16x32_bf16 a[52:55], a[244:247], v[84:87], a[52:55]\n"
      "v_exp_f32 v21, v57\n"
      "v_mfma_f32_16x16x32_bf16 a[56:59], a[248:251], v[84:87], a[56:59]\n"
      "v_exp_f32 v22, v58\n"
      "v_mfma_f32_16x16x32_bf16 a[60:63], a[252:255], v[84:87], a[60:63]\n"
      "buffer_load_dwordx4 v1, s[20:23], s64 offen offset:0 nt lds\n"
      "s_nop 7\n"
      "v_mfma_f32_16x16x32_bf16 v[64:67], a[192:195], v[224:227], v[120:123]\n"
      "v_exp_f32 v23, v59\n"
      "v_mfma_f32_16x16x32_bf16 v[72:75], a[192:195], v[240:243], v[124:127]\n"
      "v_cvt_pk_bf16_f32 v90, v24, v25\n"
      "v_cvt_pk_bf16_f32 v91, v156, v157\n"
      "v_mfma_f32_16x16x32_bf16 v[68:71], a[196:199], v[224:227], v[120:123]\n"
      "v_exp_f32 v24, v60\n"
      "v_mfma_f32_16x16x32_bf16 v[136:139], v[152:155], v[88:91], v[136:139]\n"
      "v_exp_f32 v25, v61\n"
      "v_mfma_f32_16x16x32_bf16 v[76:79], a[196:199], v[240:243], v[124:127]\n"
      "v_exp_f32 v156, v62\n"
      "s_nop 7\n"
      "v_mfma_f32_16x16x32_bf16 v[64:67], a[200:203], v[228:231], v[64:67]\n"
      "v_exp_f32 v157, v63\n"
      "v_mfma_f32_16x16x32_bf16 v[72:75], a[200:203], v[244:247], v[72:75]\n"
      "buffer_load_dwordx4 v2, s[20:23], s64 offen offset:1024 nt lds\n"
      "v_mfma_f32_16x16x32_bf16 v[68:71], a[204:207], v[228:231], v[68:71]\n"
      "v_cvt_pk_bf16_f32 v92, v20, v21\n"
      "v_cvt_pk_bf16_f32 v93, v22, v23\n"
      "v_mfma_f32_16x16x32_bf16 v[76:79], a[204:207], v[244:247], v[76:79]\n"
      "v_cvt_pk_bf16_f32 v94, v24, v25\n"
      "v_cvt_pk_bf16_f32 v95, v156, v157\n"
      "s_nop 7\n"
      "v_mfma_f32_16x16x32_bf16 v[64:67], a[208:211], v[232:235], v[64:67]\n"
      "v_mfma_f32_16x16x32_bf16 v[140:143], v[152:155], v[92:95], v[140:143]\n"
      "v_mfma_f32_16x16x32_bf16 v[72:75], a[208:211], v[248:251], v[72:75]\n"
      "buffer_load_dwordx4 v3, s[20:23], s64 offen offset:2048 nt lds\n"
      "v_mfma_f32_16x16x32_bf16 v[68:71], a[212:215], v[232:235], v[68:71]\n"
      "v_mfma_f32_16x16x32_bf16 v[76:79], a[212:215], v[248:251], v[76:79]\n"
      "s_waitcnt vmcnt(15)\n"
      "s_nop 7\n"
      "v_mfma_f32_16x16x32_bf16 v[64:67], a[216:219], v[236:239], v[64:67]\n"
      "ds_read_b128 a[192:195], v5 offset:16384\n"
      "ds_read_b128 a[196:199], v6 offset:16384\n"
      "v_mfma_f32_16x16x32_bf16 v[72:75], a[216:219], v[252:255], v[72:75]\n"
      "ds_read_b128 a[200:203], v5 offset:16896\n"
      "ds_read_b128 a[204:207], v6 offset:16896\n"
      "v_mfma_f32_16x16x32_bf16 v[68:71], a[220:223], v[236:239], v[68:71]\n"
      "ds_read_b128 a[208:211], v5 offset:17408\n"
      "ds_read_b128 a[212:215], v6 offset:17408\n"
      "v_mfma_f32_16x16x32_bf16 v[76:79], a[220:223], v[252:255], v[76:79]\n"
      "ds_read_b128 a[216:219], v5 offset:17920\n"
      "s_nop 7\n"
      "v_mfma_f32_16x16x32_bf16 a[64:67], a[224:227], v[88:91], a[64:67]\n"
      "ds_read_b128 a[220:223], v6 offset:17920\n"
      "v_mfma_f32_16x16x32_bf16 a[68:71], a[228:231], v[88:91], a[68:71]\n"
      "v_exp_f32 v20, v64\n"
      "v_mfma_f32_16x16x32_bf16 a[72:75], a[232:235], v[88:91], a[72:75]\n"
      "buffer_load_dwordx4 v4, s[20:23], s64 offen offset:3072 nt lds\n"
      "v_mfma_f32_16x16x32_bf16 a[76:79], a[236:239], v[88:91], a[76:79]\n"
      "v_exp_f32 v21, v65\n"
      "s_nop 7\n"
      "v_mfma_f32_16x16x32_bf16 a[80:83], a[240:243], v[88:91], a[80:83]\n"
      "v_exp_f32 v22, v66\n"
      "v_mfma_f32_16x16x32_bf16 a[84:87], a[244:247], v[88:91], a[84:87]\n"
      "s_add_u32 m0, s40, 0x2000\n"
      "v_cvt_pk_bf16_f32 v96, v20, v21\n"
      "v_mfma_f32_16x16x32_bf16 a[88:91], a[248:251], v[88:91], a[88:91]\n"
      "v_exp_f32 v23, v67\n"
      "v_mfma_f32_16x16x32_bf16 a[92:95], a[252:255], v[88:91], a[92:95]\n"
      "v_exp_f32 v24, v68\n"
      "s_nop 7\n"
      "v_mfma_f32_16x16x32_bf16 a[96:99], a[224:227], v[92:95], a[96:99]\n"
      "v_exp_f32 v25, v69\n"
      "v_mfma_f32_16x16x32_bf16 a[100:103], a[228:231], v[92:95], a[100:103]\n"
      "v_exp_f32 v156, v70\n"
      "v_mfma_f32_16x16x32_bf16 a[104:107], a[232:235], v[92:95], a[104:107]\n"
      "buffer_load_dwordx4 v1, s[24:27], s63 offen offset:0 nt lds\n"
      "v_mfma_f32_16x16x32_bf16 a[108:111], a[236:239], v[92:95], a[108:111]\n"
      "v_exp_f32 v157, v71\n"
      "s_nop 7\n"
      "v_mfma_f32_16x16x32_bf16 a[112:115], a[240:243], v[92:95], a[112:115]\n"
      "v_exp_f32 v20, v72\n"
      "v_mfma_f32_16x16x32_bf16 a[116:119], a[244:247], v[92:95], a[116:119]\n"
      "v_cvt_pk_bf16_f32 v97, v22, v23\n"
      "v_cvt_pk_bf16_f32 v98, v24, v25\n"
      "v_mfma_f32_16x16x32_bf16 a[120:123], a[248:251], v[92:95], a[120:123]\n"
      "v_cvt_pk_bf16_f32 v99, v156, v157\n"
      "s_waitcnt lgkmcnt(0)\n"
      "v_mfma_f32_16x16x32_bf16 a[124:127], a[252:255], v[92:95], a[124:127]\n"
      "v_exp_f32 v21, v73\n"
      "v_mfma_f32_16x16x32_bf16 v[144:147], v[152:155], v[96:99], v[144:147]\n"
      "v_exp_f32 v22, v74\n"
      "s_nop 7\n"
      "v_mfma_f32_16x16x32_bf16 v[32:35], a[192:195], v[160:163], v[104:107]\n"
      "buffer_load_dwordx4 v2, s[24:27], s63 offen offset:1024 nt lds\n"
      "v_mfma_f32_16x16x32_bf16 v[40:43], a[192:195], v[176:179], v[108:111]\n"
      "v_exp_f32 v23, v75\n"
      "v_mfma_f32_16x16x32_bf16 v[36:39], a[196:199], v[160:163], v[104:107]\n"
      "v_exp_f32 v24, v76\n"
      "v_mfma_f32_16x16x32_bf16 v[44:47], a[196:199], v[176:179], v[108:111]\n"
      "v_exp_f32 v25, v77\n"
      "s_nop 7\n"
      "v_mfma_f32_16x16x32_bf16 v[32:35], a[200:203], v[164:167], v[32:35]\n"
      "v_exp_f32 v156, v78\n"
      "v_mfma_f32_16x16x32_bf16 v[40:43], a[200:203], v[180:183], v[40:43]\n"
      "v_exp_f32 v157, v79\n"
      "v_mfma_f32_16x16x32_bf16 v[36:39], a[204:207], v[164:167], v[36:39]\n"
      "buffer_load_dwordx4 v3, s[24:27], s63 offen offset:2048 nt lds\n"
      "v_mfma_f32_16x16x32_bf16 v[44:47], a[204:207], v[180:183], v[44:47]\n"
      "v_cvt_pk_bf16_f32 v100, v20, v21\n"
      "v_cvt_pk_bf16_f32 v101, v22, v23\n"
      "s_nop 7\n"
      "v_mfma_f32_16x16x32_bf16 v[32:35], a[208:211], v[168:171], v[32:35]\n"
      "v_cvt_pk_bf16_f32 v102, v24, v25\n"
      "v_cvt_pk_bf16_f32 v103, v156, v157\n"
      "v_mfma_f32_16x16x32_bf16 v[40:43], a[208:211], v[184:187], v[40:43]\n"
      "s_nop 0\n"
      "v_mfma_f32_16x16x32_bf16 v[148:151], v[152:155], v[100:103], v[148:151]\n"
      "v_mfma_f32_16x16x32_bf16 v[36:39], a[212:215], v[168:171], v[36:39]\n"
      "buffer_load_dwordx4 v4, s[24:27], s63 offen offset:3072 nt lds\n"
      "v_mfma_f32_16x16x32_bf16 v[44:47], a[212:215], v[184:187], v[44:47]\n"
      "s_add_u32 m0, s40, 0x3000\n"
      "s_nop 7\n"
      "v_mfma_f32_16x16x32_bf16 v[32:35], a[216:219], v[172:175], v[32:35]\n"
      "buffer_load_dwordx4 v1, s[24:27], s64 offen offset:0 nt lds\n"
      "v_mfma_f32_16x16x32_bf16 v[40:43], a[216:219], v[188:191], v[40:43]\n"
      "v_mfma_f32_16x16x32_bf16 v[36:39], a[220:223], v[172:175], v[36:39]\n"
      "v_mfma_f32_16x16x32_bf16 v[44:47], a[220:223], v[188:191], v[44:47]\n"
      "s_nop 7\n"
      "v_mfma_f32_16x16x32_bf16 a[128:131], a[224:227], v[96:99], a[128:131]\n"
      "v_mfma_f32_16x16x32_bf16 a[132:135], a[228:231], v[96:99], a[132:135]\n"
      "v_exp_f32 v20, v32\n"
      "v_mfma_f32_16x16x32_bf16 a[136:139], a[232:235], v[96:99], a[136:139]\n"
      "v_exp_f32 v21, v33\n"
      "v_mfma_f32_16x16x32_bf16 a[140:143], a[236:239], v[96:99], a[140:143]\n"
      "v_exp_f32 v22, v34\n"
      "s_nop 7\n"
      "v_mfma_f32_16x16x32_bf16 a[144:147], a[240:243], v[96:99], a[144:147]\n"
      "v_exp_f32 v23, v35\n"
      "v_mfma_f32_16x16x32_bf16 a[148:151], a[244:247], v[96:99], a[148:151]\n"
      "v_exp_f32 v24, v36\n"
      "v_mfma_f32_16x16x32_bf16 a[152:155], a[248:251], v[96:99], a[152:155]\n"
      "v_exp_f32 v25, v37\n"
      "v_mfma_f32_16x16x32_bf16 a[156:159], a[252:255], v[96:99], a[156:159]\n"
      "v_exp_f32 v156, v38\n"
      "s_nop 7\n"
      "v_mfma_f32_16x16x32_bf16 a[160:163], a[224:227], v[100:103], a[160:163]\n"
      "v_exp_f32 v157, v39\n"
      "v_mfma_f32_16x16x32_bf16 a[164:167], a[228:231], v[100:103], a[164:167]\n"
      "v_cvt_pk_bf16_f32 v80, v20, v21\n"
      "s_waitcnt vmcnt(13)\n"
      "v_mfma_f32_16x16x32_bf16 a[168:171], a[232:235], v[100:103], a[168:171]\n"
      "v_cvt_pk_bf16_f32 v81, v22, v23\n"
      "v_cvt_pk_bf16_f32 v82, v24, v25\n"
      "v_mfma_f32_16x16x32_bf16 a[172:175], a[236:239], v[100:103], a[172:175]\n"
      "buffer_load_dwordx4 v2, s[24:27], s64 offen offset:1024 nt lds\n"
      "s_nop 7\n"
      "v_mfma_f32_16x16x32_bf16 a[176:179], a[240:243], v[100:103], a[176:179]\n"
      "v_exp_f32 v20, v40\n"
      "v_mfma_f32_16x16x32_bf16 a[180:183], a[244:247], v[100:103], a[180:183]\n"
      "v_exp_f32 v21, v41\n"
      "v_mfma_f32_16x16x32_bf16 a[184:187], a[248:251], v[100:103], a[184:187]\n"
      "ds_read_b64_tr_b16 a[224:225], v7 offset:16384\n"
      "v_cvt_pk_bf16_f32 v83, v156, v157\n"
      "v_mfma_f32_16x16x32_bf16 a[188:191], a[252:255], v[100:103], a[188:191]\n"
      "v_exp_f32 v22, v42\n"
      "v_mfma_f32_16x16x32_bf16 v[128:131], v[152:155], v[80:83], v[128:131]\n"
      "ds_read_b64_tr_b16 a[226:227], v8 offset:16384\n"
      "v_cvt_pk_bf16_f32 v84, v20, v21\n"
      "s_nop 7\n"
      "v_mfma_f32_16x16x32_bf16 v[48:51], a[192:195], v[192:195], v[112:115]\n"
      "v_exp_f32 v23, v43\n"
      "v_mfma_f32_16x16x32_bf16 v[56:59], a[192:195], v[208:211], v[116:119]\n"
      "buffer_load_dwordx4 v3, s[24:27], s64 offen offset:2048 nt lds\n"
      "v_mfma_f32_16x16x32_bf16 v[52:55], a[196:199], v[192:195], v[112:115]\n"
      "ds_read_b64_tr_b16 a[228:229], v9 offset:16384\n"
      "ds_read_b64_tr_b16 a[230:231], v10 offset:16384\n"
      "v_mfma_f32_16x16x32_bf16 v[60:63], a[196:199], v[208:211], v[116:119]\n"
      "ds_read_b64_tr_b16 a[232:233], v7 offset:16896\n"
      "ds_read_b64_tr_b16 a[234:235], v8 offset:16896\n"
      "v_mfma_f32_16x16x32_bf16 v[48:51], a[200:203], v[196:199], v[48:51]\n"
      "ds_read_b64_tr_b16 a[236:237], v9 offset:16896\n"
      "ds_read_b64_tr_b16 a[238:239], v10 offset:16896\n"
      "v_mfma_f32_16x16x32_bf16 v[56:59], a[200:203], v[212:215], v[56:59]\n"
      "v_exp_f32 v24, v44\n"
      "v_mfma_f32_16x16x32_bf16 v[52:55], a[204:207], v[196:199], v[52:55]\n"
      "ds_read_b64_tr_b16 a[240:241], v7 offset:17408\n"
      "ds_read_b64_tr_b16 a[242:243], v8 offset:17408\n"
      "v_mfma_f32_16x16x32_bf16 v[60:63], a[204:207], v[212:215], v[60:63]\n"
      "buffer_load_dwordx4 v4, s[24:27], s64 offen offset:3072 nt lds\n"
      "s_nop 7\n"
      "v_mfma_f32_16x16x32_bf16 v[48:51], a[208:211], v[200:203], v[48:51]\n"
      "s_add_u32 m0, s40, 0x4000\n"
      "s_add_u32 s63, s63, 0x8000\n"
      "v_mfma_f32_16x16x32_bf16 v[56:59], a[208:211], v[216:219], v[56:59]\n"
      "ds_read_b64_tr_b16 a[244:245], v9 offset:17408\n"
      "ds_read_b64_tr_b16 a[246:247], v10 offset:17408\n"
      "v_mfma_f32_16x16x32_bf16 v[52:55], a[212:215], v[200:203], v[52:55]\n"
      "v_exp_f32 v25, v45\n"
      "v_mfma_f32_16x16x32_bf16 v[60:63], a[212:215], v[216:219], v[60:63]\n"
      "ds_read_b64_tr_b16 a[248:249], v7 offset:17920\n"
      "ds_read_b64_tr_b16 a[250:251], v8 offset:17920\n"
      "v_mfma_f32_16x16x32_bf16 v[48:51], a[216:219], v[204:207], v[48:51]\n"
      "ds_read_b64_tr_b16 a[252:253], v9 offset:17920\n"
      "v_cvt_pk_bf16_f32 v85, v22, v23\n"
      "v_mfma_f32_16x16x32_bf16 v[56:59], a[216:219], v[220:223], v[56:59]\n"
      "ds_read_b64_tr_b16 a[254:255], v10 offset:17920\n"
      "v_cvt_pk_bf16_f32 v86, v24, v25\n"
      "v_mfma_f32_16x16x32_bf16 v[52:55], a[220:223], v[204:207], v[52:55]\n"
      "v_exp_f32 v156, v46\n"
      "v_mfma_f32_16x16x32_bf16 v[60:63], a[220:223], v[220:223], v[60:63]\n"
      "s_waitcnt lgkmcnt(8)\n"
      "s_nop 7\n"
      "v_mfma_f32_16x16x32_bf16 a[0:3], a[224:227], v[80:83], a[0:3]\n"
      "buffer_load_dwordx4 v1, s[20:23], s63 offen offset:0 nt lds\n"
      "v_mfma_f32_16x16x32_bf16 a[4:7], a[228:231], v[80:83], a[4:7]\n"
      "v_exp_f32 v157, v47\n"
      "v_mfma_f32_16x16x32_bf16 a[8:11], a[232:235], v[80:83], a[8:11]\n"
      "s_waitcnt lgkmcnt(0)\n"
      "v_mfma_f32_16x16x32_bf16 a[12:15], a[236:239], v[80:83], a[12:15]\n"
      "v_cvt_pk_bf16_f32 v87, v156, v157\n"
      "v_exp_f32 v20, v48\n"
      "v_exp_f32 v21, v49\n"
      "v_mfma_f32_16x16x32_bf16 a[16:19], a[240:243], v[80:83], a[16:19]\n"
      "v_exp_f32 v22, v50\n"
      "v_mfma_f32_16x16x32_bf16 v[132:135], v[152:155], v[84:87], v[132:135]\n"
      "v_exp_f32 v23, v51\n"
      "v_mfma_f32_16x16x32_bf16 a[20:23], a[244:247], v[80:83], a[20:23]\n"
      "v_exp_f32 v24, v52\n"
      "v_mfma_f32_16x16x32_bf16 a[24:27], a[248:251], v[80:83], a[24:27]\n"
      "buffer_load_dwordx4 v2, s[20:23], s63 offen offset:1024 nt lds\n"
      "v_mfma_f32_16x16x32_bf16 a[28:31], a[252:255], v[80:83], a[28:31]\n"
      "v_exp_f32 v25, v53\n"
      "s_nop 7\n"
      "v_mfma_f32_16x16x32_bf16 a[32:35], a[224:227], v[84:87], a[32:35]\n"
      "v_exp_f32 v156, v54\n"
      "v_mfma_f32_16x16x32_bf16 a[36:39], a[228:231], v[84:87], a[36:39]\n"
      "v_cvt_pk_bf16_f32 v88, v20, v21\n"
      "v_cvt_pk_bf16_f32 v89, v22, v23\n"
      "v_mfma_f32_16x16x32_bf16 a[40:43], a[232:235], v[84:87], a[40:43]\n"
      "v_exp_f32 v157, v55\n"
      "v_mfma_f32_16x16x32_bf16 a[44:47], a[236:239], v[84:87], a[44:47]\n"
      "v_exp_f32 v20, v56\n"
      "v_mfma_f32_16x16x32_bf16 a[48:51], a[240:243], v[84:87], a[48:51]\n"
      "buffer_load_dwordx4 v3, s[20:23], s63 offen offset:2048 nt lds\n"
      "v_mfma_f32_16x16x32_bf16 a[52:55], a[244:247], v[84:87], a[52:55]\n"
      "v_exp_f32 v21, v57\n"
      "v_mfma_f32_16x16x32_bf16 a[56:59], a[248:251], v[84:87], a[56:59]\n"
      "v_exp_f32 v22, v58\n"
      "v_mfma_f32_16x16x32_bf16 a[60:63], a[252:255], v[84:87], a[60:63]\n"
      "v_exp_f32 v23, v59\n"
      "s_nop 7\n"
      "v_mfma_f32_16x16x32_bf16 v[64:67], a[192:195], v[224:227], v[120:123]\n"
      "v_cvt_pk_bf16_f32 v90, v24, v25\n"
      "v_cvt_pk_bf16_f32 v91, v156, v157\n"
      "v_mfma_f32_16x16x32_bf16 v[72:75], a[192:195], v[240:243], v[124:127]\n"
      "v_exp_f32 v24, v60\n"
      "v_mfma_f32_16x16x32_bf16 v[136:139], v[152:155], v[88:91], v[136:139]\n"
      "v_exp_f32 v25, v61\n"
      "v_mfma_f32_16x16x32_bf16 v[68:71], a[196:199], v[224:227], v[120:123]\n"
      "buffer_load_dwordx4 v4, s[20:23], s63 offen offset:3072 nt lds\n"
      "v_mfma_f32_16x16x32_bf16 v[76:79], a[196:199], v[240:243], v[124:127]\n"
      "s_add_u32 m0, s40, 0x5000\n"
      "s_add_u32 s64, s64, 0x8000\n"
      "v_mfma_f32_16x16x32_bf16 v[64:67], a[200:203], v[228:231], v[64:67]\n"
      "v_exp_f32 v156, v62\n"
      "v_mfma_f32_16x16x32_bf16 v[72:75], a[200:203], v[244:247], v[72:75]\n"
      "v_exp_f32 v157, v63\n"
      "v_mfma_f32_16x16x32_bf16 v[68:71], a[204:207], v[228:231], v[68:71]\n"
      "v_cvt_pk_bf16_f32 v92, v20, v21\n"
      "v_cvt_pk_bf16_f32 v93, v22, v23\n"
      "v_mfma_f32_16x16x32_bf16 v[76:79], a[204:207], v[244:247], v[76:79]\n"
      "v_cvt_pk_bf16_f32 v94, v24, v25\n"
      "v_cvt_pk_bf16_f32 v95, v156, v157\n"
      "s_nop 7\n"
      "v_mfma_f32_16x16x32_bf16 v[64:67], a[208:211], v[232:235], v[64:67]\n"
      "buffer_load_dwordx4 v1, s[20:23], s64 offen offset:0 nt lds\n"
      "v_mfma_f32_16x16x32_bf16 v[140:143], v[152:155], v[92:95], v[140:143]\n"
      "v_mfma_f32_16x16x32_bf16 v[72:75], a[208:211], v[248:251], v[72:75]\n"
      "v_mfma_f32_16x16x32_bf16 v[68:71], a[212:215], v[232:235], v[68:71]\n"
      "v_mfma_f32_16x16x32_bf16 v[76:79], a[212:215], v[248:251], v[76:79]\n"
      "s_waitcnt vmcnt(13)\n"
      "v_mfma_f32_16x16x32_bf16 v[64:67], a[216:219], v[236:239], v[64:67]\n"
      "ds_read_b128 a[192:195], v5 offset:0\n"
      "ds_read_b128 a[196:199], v6 offset:0\n"
      "v_mfma_f32_16x16x32_bf16 v[72:75], a[216:219], v[252:255], v[72:75]\n"
      "ds_read_b128 a[200:203], v5 offset:512\n"
      "ds_read_b128 a[204:207], v6 offset:512\n"
      "v_mfma_f32_16x16x32_bf16 v[68:71], a[220:223], v[236:239], v[68:71]\n"
      "ds_read_b128 a[208:211], v5 offset:1024\n"
      "ds_read_b128 a[212:215], v6 offset:1024\n"
      "v_mfma_f32_16x16x32_bf16 v[76:79], a[220:223], v[252:255], v[76:79]\n"
      "ds_read_b128 a[216:219], v5 offset:1536\n"
      "s_nop 7\n"
      "v_mfma_f32_16x16x32_bf16 a[64:67], a[224:227], v[88:91], a[64:67]\n"
      "ds_read_b128 a[220:223], v6 offset:1536\n"
      "v_mfma_f32_16x16x32_bf16 a[68:71], a[228:231], v[88:91], a[68:71]\n"
      "v_exp_f32 v20, v64\n"
      "v_mfma_f32_16x16x32_bf16 a[72:75], a[232:235], v[88:91], a[72:75]\n"
      "buffer_load_dwordx4 v2, s[20:23], s64 offen offset:1024 nt lds\n"
      "v_mfma_f32_16x16x32_bf16 a[76:79], a[236:239], v[88:91], a[76:79]\n"
      "v_exp_f32 v21, v65\n"
      "v_mfma_f32_16x16x32_bf16 a[80:83], a[240:243], v[88:91], a[80:83]\n"
      "v_exp_f32 v22, v66\n"
      "v_mfma_f32_16x16x32_bf16 a[84:87], a[244:247], v[88:91], a[84:87]\n"
      "v_exp_f32 v23, v67\n"
      "v_mfma_f32_16x16x32_bf16 a[88:91], a[248:251], v[88:91], a[88:91]\n"
      "v_exp_f32 v24, v68\n"
      "v_mfma_f32_16x16x32_bf16 a[92:95], a[252:255], v[88:91], a[92:95]\n"
      "buffer_load_dwordx4 v3, s[20:23], s64 offen offset:2048 nt lds\n"
      "s_nop 7\n"
      "v_mfma_f32_16x16x32_bf16 a[96:99], a[224:227], v[92:95], a[96:99]\n"
      "v_exp_f32 v25, v69\n"
      "v_mfma_f32_16x16x32_bf16 a[100:103], a[228:231], v[92:95], a[100:103]\n"
      "v_cvt_pk_bf16_f32 v96, v20, v21\n"
      "v_cvt_pk_bf16_f32 v97, v22, v23\n"
      "v_mfma_f32_16x16x32_bf16 a[104:107], a[232:235], v[92:95], a[104:107]\n"
      "v_exp_f32 v156, v70\n"
      "v_mfma_f32_16x16x32_bf16 a[108:111], a[236:239], v[92:95], a[108:111]\n"
      "v_exp_f32 v157, v71\n"
      "v_mfma_f32_16x16x32_bf16 a[112:115], a[240:243], v[92:95], a[112:115]\n"
      "v_exp_f32 v20, v72\n"
      "v_mfma_f32_16x16x32_bf16 a[116:119], a[244:247], v[92:95], a[116:119]\n"
      "buffer_load_dwordx4 v4, s[20:23], s64 offen offset:3072 nt lds\n"
      "v_mfma_f32_16x16x32_bf16 a[120:123], a[248:251], v[92:95], a[120:123]\n"
      "v_cvt_pk_bf16_f32 v98, v24, v25\n"
      "s_waitcnt lgkmcnt(0)\n"
      "v_mfma_f32_16x16x32_bf16 a[124:127], a[252:255], v[92:95], a[124:127]\n"
      "s_add_u32 m0, s40, 0x6000\n"
      "v_cvt_pk_bf16_f32 v99, v156, v157\n"
      "s_nop 7\n"
      "v_mfma_f32_16x16x32_bf16 v[32:35], a[192:195], v[160:163], v[104:107]\n"
      "v_exp_f32 v21, v73\n"
      "v_mfma_f32_16x16x32_bf16 v[144:147], v[152:155], v[96:99], v[144:147]\n"
      "v_exp_f32 v22, v74\n"
      "v_mfma_f32_16x16x32_bf16 v[40:43], a[192:195], v[176:179], v[108:111]\n"
      "v_exp_f32 v23, v75\n"
      "v_mfma_f32_16x16x32_bf16 v[36:39], a[196:199], v[160:163], v[104:107]\n"
      "v_exp_f32 v24, v76\n"
      "v_mfma_f32_16x16x32_bf16 v[44:47], a[196:199], v[176:179], v[108:111]\n"
      "v_exp_f32 v25, v77\n"
      "v_mfma_f32_16x16x32_bf16 v[32:35], a[200:203], v[164:167], v[32:35]\n"
      "v_exp_f32 v156, v78\n"
      "v_mfma_f32_16x16x32_bf16 v[40:43], a[200:203], v[180:183], v[40:43]\n"
      "v_exp_f32 v157, v79\n"
      "v_mfma_f32_16x16x32_bf16 v[36:39], a[204:207], v[164:167], v[36:39]\n"
      "buffer_load_dwordx4 v1, s[24:27], s63 offen offset:0 nt lds\n"
      "v_mfma_f32_16x16x32_bf16 v[44:47], a[204:207], v[180:183], v[44:47]\n"
      "v_cvt_pk_bf16_f32 v100, v20, v21\n"
      "v_cvt_pk_bf16_f32 v101, v22, v23\n"
      "s_nop 7\n"
      "v_mfma_f32_16x16x32_bf16 v[32:35], a[208:211], v[168:171], v[32:35]\n"
      "v_cvt_pk_bf16_f32 v102, v24, v25\n"
      "v_cvt_pk_bf16_f32 v103, v156, v157\n"
      "v_mfma_f32_16x16x32_bf16 v[40:43], a[208:211], v[184:187], v[40:43]\n"
      "s_nop 0\n"
      "v_mfma_f32_16x16x32_bf16 v[148:151], v[152:155], v[100:103], v[148:151]\n"
      "v_mfma_f32_16x16x32_bf16 v[36:39], a[212:215], v[168:171], v[36:39]\n"
      "buffer_load_dwordx4 v2, s[24:27], s63 offen offset:1024 nt lds\n"
      "v_mfma_f32_16x16x32_bf16 v[44:47], a[212:215], v[184:187], v[44:47]\n"
      "v_mfma_f32_16x16x32_bf16 v[32:35], a[216:219], v[172:175], v[32:35]\n"
      "v_mfma_f32_16x16x32_bf16 v[40:43], a[216:219], v[188:191], v[40:43]\n"
      "v_mfma_f32_16x16x32_bf16 v[36:39], a[220:223], v[172:175], v[36:39]\n"
      "buffer_load_dwordx4 v3, s[24:27], s63 offen offset:2048 nt lds\n"
      "v_mfma_f32_16x16x32_bf16 v[44:47], a[220:223], v[188:191], v[44:47]\n"
      "s_nop 7\n"
      "v_mfma_f32_16x16x32_bf16 a[128:131], a[224:227], v[96:99], a[128:131]\n"
      "v_mfma_f32_16x16x32_bf16 a[132:135], a[228:231], v[96:99], a[132:135]\n"
      "v_exp_f32 v20, v32\n"
      "v_mfma_f32_16x16x32_bf16 a[136:139], a[232:235], v[96:99], a[136:139]\n"
      "v_exp_f32 v21, v33\n"
      "v_mfma_f32_16x16x32_bf16 a[140:143], a[236:239], v[96:99], a[140:143]\n"
      "v_exp_f32 v22, v34\n"
      "v_mfma_f32_16x16x32_bf16 a[144:147], a[240:243], v[96:99], a[144:147]\n"
      "v_exp_f32 v23, v35\n"
      "v_mfma_f32_16x16x32_bf16 a[148:151], a[244:247], v[96:99], a[148:151]\n"
      "v_exp_f32 v24, v36\n"
      "v_mfma_f32_16x16x32_bf16 a[152:155], a[248:251], v[96:99], a[152:155]\n"
      "v_exp_f32 v25, v37\n"
      "v_mfma_f32_16x16x32_bf16 a[156:159], a[252:255], v[96:99], a[156:159]\n"
      "v_exp_f32 v156, v38\n"
      "s_nop 7\n"
      "v_mfma_f32_16x16x32_bf16 a[160:163], a[224:227], v[100:103], a[160:163]\n"
      "buffer_load_dwordx4 v4, s[24:27], s63 offen offset:3072 nt lds\n"
      "v_mfma_f32_16x16x32_bf16 a[164:167], a[228:231], v[100:103], a[164:167]\n"
      "v_cvt_pk_bf16_f32 v80, v20, v21\n"
      "s_waitcnt vmcnt(12)\n"
      "v_mfma_f32_16x16x32_bf16 a[168:171], a[232:235], v[100:103], a[168:171]\n"
      "v_cvt_pk_bf16_f32 v81, v22, v23\n"
      "s_add_u32 m0, s40, 0x7000\n"
      "v_mfma_f32_16x16x32_bf16 a[172:175], a[236:239], v[100:103], a[172:175]\n"
      "v_exp_f32 v157, v39\n"
      "v_mfma_f32_16x16x32_bf16 a[176:179], a[240:243], v[100:103], a[176:179]\n"
      "v_cvt_pk_bf16_f32 v82, v24, v25\n"
      "v_cvt_pk_bf16_f32 v83, v156, v157\n"
      "v_mfma_f32_16x16x32_bf16 a[180:183], a[244:247], v[100:103], a[180:183]\n"
      "v_exp_f32 v20, v40\n"
      "v_mfma_f32_16x16x32_bf16 v[128:131], v[152:155], v[80:83], v[128:131]\n"
      "v_exp_f32 v21, v41\n"
      "v_mfma_f32_16x16x32_bf16 a[184:187], a[248:251], v[100:103], a[184:187]\n"
      "v_exp_f32 v22, v42\n"
      "v_mfma_f32_16x16x32_bf16 a[188:191], a[252:255], v[100:103], a[188:191]\n"
      "ds_read_b64_tr_b16 a[224:225], v7 offset:0\n"
      "ds_read_b64_tr_b16 a[226:227], v8 offset:0\n"
      "s_nop 7\n"
      "v_mfma_f32_16x16x32_bf16 v[48:51], a[192:195], v[192:195], v[112:115]\n"
      "v_exp_f32 v23, v43\n"
      "v_mfma_f32_16x16x32_bf16 v[56:59], a[192:195], v[208:211], v[116:119]\n"
      "buffer_load_dwordx4 v1, s[24:27], s64 offen offset:0 nt lds\n"
      "v_mfma_f32_16x16x32_bf16 v[52:55], a[196:199], v[192:195], v[112:115]\n"
      "ds_read_b64_tr_b16 a[228:229], v9 offset:0\n"
      "ds_read_b64_tr_b16 a[230:231], v10 offset:0\n"
      "v_mfma_f32_16x16x32_bf16 v[60:63], a[196:199], v[208:211], v[116:119]\n"
      "ds_read_b64_tr_b16 a[232:233], v7 offset:512\n"
      "ds_read_b64_tr_b16 a[234:235], v8 offset:512\n"
      "v_mfma_f32_16x16x32_bf16 v[48:51], a[200:203], v[196:199], v[48:51]\n"
      "ds_read_b64_tr_b16 a[236:237], v9 offset:512\n"
      "v_cvt_pk_bf16_f32 v84, v20, v21\n"
      "v_mfma_f32_16x16x32_bf16 v[56:59], a[200:203], v[212:215], v[56:59]\n"
      "ds_read_b64_tr_b16 a[238:239], v10 offset:512\n"
      "ds_read_b64_tr_b16 a[240:241], v7 offset:1024\n"
      "v_mfma_f32_16x16x32_bf16 v[52:55], a[204:207], v[196:199], v[52:55]\n"
      "v_exp_f32 v24, v44\n"
      "v_mfma_f32_16x16x32_bf16 v[60:63], a[204:207], v[212:215], v[60:63]\n"
      "buffer_load_dwordx4 v2, s[24:27], s64 offen offset:1024 nt lds\n"
      "s_nop 7\n"
      "v_mfma_f32_16x16x32_bf16 v[48:51], a[208:211], v[200:203], v[48:51]\n"
      "ds_read_b64_tr_b16 a[242:243], v8 offset:1024\n"
      "ds_read_b64_tr_b16 a[244:245], v9 offset:1024\n"
      "v_mfma_f32_16x16x32_bf16 v[56:59], a[208:211], v[216:219], v[56:59]\n"
      "ds_read_b64_tr_b16 a[246:247], v10 offset:1024\n"
      "ds_read_b64_tr_b16 a[248:249], v7 offset:1536\n"
      "v_mfma_f32_16x16x32_bf16 v[52:55], a[212:215], v[200:203], v[52:55]\n"
      "v_exp_f32 v25, v45\n"
      "v_mfma_f32_16x16x32_bf16 v[60:63], a[212:215], v[216:219], v[60:63]\n"
      "ds_read_b64_tr_b16 a[250:251], v8 offset:1536\n"
      "v_cvt_pk_bf16_f32 v85, v22, v23\n"
      "v_mfma_f32_16x16x32_bf16 v[48:51], a[216:219], v[204:207], v[48:51]\n"
      "ds_read_b64_tr_b16 a[252:253], v9 offset:1536\n"
      "ds_read_b64_tr_b16 a[254:255], v10 offset:1536\n"
      "v_mfma_f32_16x16x32_bf16 v[56:59], a[216:219], v[220:223], v[56:59]\n"
      "buffer_load_dwordx4 v3, s[24:27], s64 offen offset:2048 nt lds\n"
      "v_mfma_f32_16x16x32_bf16 v[52:55], a[220:223], v[204:207], v[52:55]\n"
      "v_cvt_pk_bf16_f32 v86, v24, v25\n"
      "s_waitcnt lgkmcnt(8)\n"
      "v_mfma_f32_16x16x32_bf16 v[60:63], a[220:223], v[220:223], v[60:63]\n"
      "v_exp_f32 v156, v46\n"
      "s_nop 7\n"
      "v_mfma_f32_16x16x32_bf16 a[0:3], a[224:227], v[80:83], a[0:3]\n"
      "v_exp_f32 v157, v47\n"
      "v_mfma_f32_16x16x32_bf16 a[4:7], a[228:231], v[80:83], a[4:7]\n"
      "v_cvt_pk_bf16_f32 v87, v156, v157\n"
      "v_exp_f32 v20, v48\n"
      "v_mfma_f32_16x16x32_bf16 a[8:11], a[232:235], v[80:83], a[8:11]\n"
      "v_exp_f32 v21, v49\n"
      "v_mfma_f32_16x16x32_bf16 v[132:135], v[152:155], v[84:87], v[132:135]\n"
      "v_exp_f32 v22, v50\n"
      "v_mfma_f32_16x16x32_bf16 a[12:15], a[236:239], v[80:83], a[12:15]\n"
      "s_waitcnt lgkmcnt(0)\n"
      "v_mfma_f32_16x16x32_bf16 a[16:19], a[240:243], v[80:83], a[16:19]\n"
      "buffer_load_dwordx4 v4, s[24:27], s64 offen offset:3072 nt lds\n"
      "v_mfma_f32_16x16x32_bf16 a[20:23], a[244:247], v[80:83], a[20:23]\n"
      "v_exp_f32 v23, v51\n"
      "v_mfma_f32_16x16x32_bf16 a[24:27], a[248:251], v[80:83], a[24:27]\n"
      "s_mov_b32 m0, s40\n"
      "s_add_u32 s63, s63, 0x8000\n"
      "v_mfma_f32_16x16x32_bf16 a[28:31], a[252:255], v[80:83], a[28:31]\n"
      "v_exp_f32 v24, v52\n"
      "s_nop 7\n"
      "v_mfma_f32_16x16x32_bf16 a[32:35], a[224:227], v[84:87], a[32:35]\n"
      "v_exp_f32 v25, v53\n"
      "v_mfma_f32_16x16x32_bf16 a[36:39], a[228:231], v[84:87], a[36:39]\n"
      "v_cvt_pk_bf16_f32 v88, v20, v21\n"
      "v_cvt_pk_bf16_f32 v89, v22, v23\n"
      "v_mfma_f32_16x16x32_bf16 a[40:43], a[232:235], v[84:87], a[40:43]\n"
      "v_exp_f32 v156, v54\n"
      "v_mfma_f32_16x16x32_bf16 a[44:47], a[236:239], v[84:87], a[44:47]\n"
      "buffer_load_dwordx4 v1, s[20:23], s63 offen offset:0 nt lds\n"
      "v_mfma_f32_16x16x32_bf16 a[48:51], a[240:243], v[84:87], a[48:51]\n"
      "v_exp_f32 v157, v55\n"
      "v_mfma_f32_16x16x32_bf16 a[52:55], a[244:247], v[84:87], a[52:55]\n"
      "v_exp_f32 v20, v56\n"
      "v_mfma_f32_16x16x32_bf16 a[56:59], a[248:251], v[84:87], a[56:59]\n"
      "v_exp_f32 v21, v57\n"
      "v_mfma_f32_16x16x32_bf16 a[60:63], a[252:255], v[84:87], a[60:63]\n"
      "v_exp_f32 v22, v58\n"
      "s_nop 7\n"
      "v_mfma_f32_16x16x32_bf16 v[64:67], a[192:195], v[224:227], v[120:123]\n"
      "v_exp_f32 v23, v59\n"
      "v_mfma_f32_16x16x32_bf16 v[72:75], a[192:195], v[240:243], v[124:127]\n"
      "buffer_load_dwordx4 v2, s[20:23], s63 offen offset:1024 nt lds\n"
      "v_mfma_f32_16x16x32_bf16 v[68:71], a[196:199], v[224:227], v[120:123]\n"
      "v_cvt_pk_bf16_f32 v90, v24, v25\n"
      "v_cvt_pk_bf16_f32 v91, v156, v157\n"
      "v_mfma_f32_16x16x32_bf16 v[76:79], a[196:199], v[240:243], v[124:127]\n"
      "v_exp_f32 v24, v60\n"
      "v_mfma_f32_16x16x32_bf16 v[136:139], v[152:155], v[88:91], v[136:139]\n"
      "v_exp_f32 v25, v61\n"
      "v_mfma_f32_16x16x32_bf16 v[64:67], a[200:203], v[228:231], v[64:67]\n"
      "v_exp_f32 v156, v62\n"
      "v_mfma_f32_16x16x32_bf16 v[72:75], a[200:203], v[244:247], v[72:75]\n"
      "v_exp_f32 v157, v63\n"
      "v_mfma_f32_16x16x32_bf16 v[68:71], a[204:207], v[228:231], v[68:71]\n"
      "v_cvt_pk_bf16_f32 v92, v20, v21\n"
      "v_cvt_pk_bf16_f32 v93, v22, v23\n"
      "v_mfma_f32_16x16x32_bf16 v[76:79], a[204:207], v[244:247], v[76:79]\n"
      "buffer_load_dwordx4 v3, s[20:23], s63 offen offset:2048 nt lds\n"
      "s_nop 7\n"
      "v_mfma_f32_16x16x32_bf16 v[64:67], a[208:211], v[232:235], v[64:67]\n"
      "v_cvt_pk_bf16_f32 v94, v24, v25\n"
      "v_cvt_pk_bf16_f32 v95, v156, v157\n"
      "v_mfma_f32_16x16x32_bf16 v[72:75], a[208:211], v[248:251], v[72:75]\n"
      "s_nop 0\n"
      "v_mfma_f32_16x16x32_bf16 v[140:143], v[152:155], v[92:95], v[140:143]\n"
      "v_mfma_f32_16x16x32_bf16 v[68:71], a[212:215], v[232:235], v[68:71]\n"
      "v_mfma_f32_16x16x32_bf16 v[76:79], a[212:215], v[248:251], v[76:79]\n"
      "s_waitcnt vmcnt(11)\n"
      "v_mfma_f32_16x16x32_bf16 v[64:67], a[216:219], v[236:239], v[64:67]\n"
      "ds_read_b128 a[192:195], v5 offset:16384\n"
      "ds_read_b128 a[196:199], v6 offset:16384\n"
      "v_mfma_f32_16x16x32_bf16 v[72:75], a[216:219], v[252:255], v[72:75]\n"
      "buffer_load_dwordx4 v4, s[20:23], s63 offen offset:3072 nt lds\n"
      "v_mfma_f32_16x16x32_bf16 v[68:71], a[220:223], v[236:239], v[68:71]\n"
      "s_add_u32 m0, s40, 0x1000\n"
      "s_add_u32 s64, s64, 0x8000\n"
      "v_mfma_f32_16x16x32_bf16 v[76:79], a[220:223], v[252:255], v[76:79]\n"
      "ds_read_b128 a[200:203], v5 offset:16896\n"
      "ds_read_b128 a[204:207], v6 offset:16896\n"
      "s_nop 7\n"
      "v_mfma_f32_16x16x32_bf16 a[64:67], a[224:227], v[88:91], a[64:67]\n"
      "ds_read_b128 a[208:211], v5 offset:17408\n"
      "ds_read_b128 a[212:215], v6 offset:17408\n"
      "v_mfma_f32_16x16x32_bf16 a[68:71], a[228:231], v[88:91], a[68:71]\n"
      "v_exp_f32 v20, v64\n"
      "v_mfma_f32_16x16x32_bf16 a[72:75], a[232:235], v[88:91], a[72:75]\n"
      "v_exp_f32 v21, v65\n"
      "v_mfma_f32_16x16x32_bf16 a[76:79], a[236:239], v[88:91], a[76:79]\n"
      "v_exp_f32 v22, v66\n"
      "v_mfma_f32_16x16x32_bf16 a[80:83], a[240:243], v[88:91], a[80:83]\n"
      "v_exp_f32 v23, v67\n"
      "v_mfma_f32_16x16x32_bf16 a[84:87], a[244:247], v[88:91], a[84:87]\n"
      "v_cvt_pk_bf16_f32 v96, v20, v21\n"
      "v_cvt_pk_bf16_f32 v97, v22, v23\n"
      "v_mfma_f32_16x16x32_bf16 a[88:91], a[248:251], v[88:91], a[88:91]\n"
      "buffer_load_dwordx4 v1, s[20:23], s64 offen offset:0 nt lds\n"
      "v_mfma_f32_16x16x32_bf16 a[92:95], a[252:255], v[88:91], a[92:95]\n"
      "v_exp_f32 v24, v68\n"
      "s_nop 7\n"
      "v_mfma_f32_16x16x32_bf16 a[96:99], a[224:227], v[92:95], a[96:99]\n"
      "v_exp_f32 v25, v69\n"
      "v_mfma_f32_16x16x32_bf16 a[100:103], a[228:231], v[92:95], a[100:103]\n"
      "v_exp_f32 v156, v70\n"
      "v_mfma_f32_16x16x32_bf16 a[104:107], a[232:235], v[92:95], a[104:107]\n"
      "v_exp_f32 v157, v71\n"
      "v_mfma_f32_16x16x32_bf16 a[108:111], a[236:239], v[92:95], a[108:111]\n"
      "ds_read_b128 a[216:219], v5 offset:17920\n"
      "v_cvt_pk_bf16_f32 v98, v24, v25\n"
      "v_mfma_f32_16x16x32_bf16 a[112:115], a[240:243], v[92:95], a[112:115]\n"
      "buffer_load_dwordx4 v2, s[20:23], s64 offen offset:1024 nt lds\n"
      "v_mfma_f32_16x16x32_bf16 a[116:119], a[244:247], v[92:95], a[116:119]\n"
      "ds_read_b128 a[220:223], v6 offset:17920\n"
      "v_cvt_pk_bf16_f32 v99, v156, v157\n"
      "v_mfma_f32_16x16x32_bf16 a[120:123], a[248:251], v[92:95], a[120:123]\n"
      "v_exp_f32 v20, v72\n"
      "v_mfma_f32_16x16x32_bf16 v[144:147], v[152:155], v[96:99], v[144:147]\n"
      "v_exp_f32 v21, v73\n"
      "v_mfma_f32_16x16x32_bf16 a[124:127], a[252:255], v[92:95], a[124:127]\n"
      "v_exp_f32 v22, v74\n"
      "s_nop 7\n"
      "s_waitcnt lgkmcnt(1)\n"
      "v_mfma_f32_16x16x32_bf16 v[32:35], a[192:195], v[160:163], v[104:107]\n"
      "s_waitcnt lgkmcnt(0)\n"
      "v_mfma_f32_16x16x32_bf16 v[40:43], a[192:195], v[176:179], v[108:111]\n"
      "v_exp_f32 v23, v75\n"
      "v_mfma_f32_16x16x32_bf16 v[36:39], a[196:199], v[160:163], v[104:107]\n"
      "buffer_load_dwordx4 v3, s[20:23], s64 offen offset:2048 nt lds\n"
      "v_mfma_f32_16x16x32_bf16 v[44:47], a[196:199], v[176:179], v[108:111]\n"
      "v_exp_f32 v24, v76\n"
      "v_mfma_f32_16x16x32_bf16 v[32:35], a[200:203], v[164:167], v[32:35]\n"
      "v_exp_f32 v25, v77\n"
      "v_mfma_f32_16x16x32_bf16 v[40:43], a[200:203], v[180:183], v[40:43]\n"
      "v_exp_f32 v156, v78\n"
      "v_mfma_f32_16x16x32_bf16 v[36:39], a[204:207], v[164:167], v[36:39]\n"
      "v_exp_f32 v157, v79\n"
      "v_mfma_f32_16x16x32_bf16 v[44:47], a[204:207], v[180:183], v[44:47]\n"
      "v_cvt_pk_bf16_f32 v100, v20, v21\n"
      "v_cvt_pk_bf16_f32 v101, v22, v23\n"
      "s_nop 7\n"
      "v_mfma_f32_16x16x32_bf16 v[32:35], a[208:211], v[168:171], v[32:35]\n"
      "buffer_load_dwordx4 v4, s[20:23], s64 offen offset:3072 nt lds\n"
      "v_mfma_f32_16x16x32_bf16 v[40:43], a[208:211], v[184:187], v[40:43]\n"
      "s_add_u32 m0, s40, 0x2000\n"
      "v_cvt_pk_bf16_f32 v102, v24, v25\n"
      "v_mfma_f32_16x16x32_bf16 v[36:39], a[212:215], v[168:171], v[36:39]\n"
      "v_cvt_pk_bf16_f32 v103, v156, v157\n"
      "v_mfma_f32_16x16x32_bf16 v[44:47], a[212:215], v[184:187], v[44:47]\n"
      "buffer_load_dwordx4 v1, s[24:27], s63 offen offset:0 nt lds\n"
      "v_mfma_f32_16x16x32_bf16 v[148:151], v[152:155], v[100:103], v[148:151]\n"
      "v_mfma_f32_16x16x32_bf16 v[32:35], a[216:219], v[172:175], v[32:35]\n"
      "v_mfma_f32_16x16x32_bf16 v[40:43], a[216:219], v[188:191], v[40:43]\n"
      "v_mfma_f32_16x16x32_bf16 v[36:39], a[220:223], v[172:175], v[36:39]\n"
      "v_mfma_f32_16x16x32_bf16 v[44:47], a[220:223], v[188:191], v[44:47]\n"
      "buffer_load_dwordx4 v2, s[24:27], s63 offen offset:1024 nt lds\n"
      "s_nop 7\n"
      "v_mfma_f32_16x16x32_bf16 a[128:131], a[224:227], v[96:99], a[128:131]\n"
      "v_mfma_f32_16x16x32_bf16 a[132:135], a[228:231], v[96:99], a[132:135]\n"
      "v_exp_f32 v20, v32\n"
      "v_mfma_f32_16x16x32_bf16 a[136:139], a[232:235], v[96:99], a[136:139]\n"
      "v_exp_f32 v21, v33\n"
      "v_mfma_f32_16x16x32_bf16 a[140:143], a[236:239], v[96:99], a[140:143]\n"
      "v_exp_f32 v22, v34\n"
      "v_mfma_f32_16x16x32_bf16 a[144:147], a[240:243], v[96:99], a[144:147]\n"
      "v_exp_f32 v23, v35\n"
      "v_mfma_f32_16x16x32_bf16 a[148:151], a[244:247], v[96:99], a[148:151]\n"
      "v_exp_f32 v24, v36\n"
      "v_mfma_f32_16x16x32_bf16 a[152:155], a[248:251], v[96:99], a[152:155]\n"
      "v_exp_f32 v25, v37\n"
      "v_mfma_f32_16x16x32_bf16 a[156:159], a[252:255], v[96:99], a[156:159]\n"
      "v_exp_f32 v156, v38\n"
      "s_nop 7\n"
      "v_mfma_f32_16x16x32_bf16 a[160:163], a[224:227], v[100:103], a[160:163]\n"
      "v_exp_f32 v157, v39\n"
      "v_mfma_f32_16x16x32_bf16 a[164:167], a[228:231], v[100:103], a[164:167]\n"
      "v_cvt_pk_bf16_f32 v80, v20, v21\n"
      "s_waitcnt vmcnt(10)\n"
      "v_mfma_f32_16x16x32_bf16 a[168:171], a[232:235], v[100:103], a[168:171]\n"
      "v_cvt_pk_bf16_f32 v81, v22, v23\n"
      "v_cvt_pk_bf16_f32 v82, v24, v25\n"
      "v_mfma_f32_16x16x32_bf16 a[172:175], a[236:239], v[100:103], a[172:175]\n"
      "v_exp_f32 v20, v40\n"
      "v_mfma_f32_16x16x32_bf16 a[176:179], a[240:243], v[100:103], a[176:179]\n"
      "v_exp_f32 v21, v41\n"
      "v_mfma_f32_16x16x32_bf16 a[180:183], a[244:247], v[100:103], a[180:183]\n"
      "buffer_load_dwordx4 v3, s[24:27], s63 offen offset:2048 nt lds\n"
      "v_mfma_f32_16x16x32_bf16 a[184:187], a[248:251], v[100:103], a[184:187]\n"
      "ds_read_b64_tr_b16 a[224:225], v7 offset:16384\n"
      "v_cvt_pk_bf16_f32 v83, v156, v157\n"
      "v_mfma_f32_16x16x32_bf16 a[188:191], a[252:255], v[100:103], a[188:191]\n"
      "v_exp_f32 v22, v42\n"
      "v_mfma_f32_16x16x32_bf16 v[128:131], v[152:155], v[80:83], v[128:131]\n"
      "ds_read_b64_tr_b16 a[226:227], v8 offset:16384\n"
      "v_cvt_pk_bf16_f32 v84, v20, v21\n"
      "s_nop 7\n"
      "v_mfma_f32_16x16x32_bf16 v[48:51], a[192:195], v[192:195], v[112:115]\n"
      "v_exp_f32 v23, v43\n"
      "v_mfma_f32_16x16x32_bf16 v[56:59], a[192:195], v[208:211], v[116:119]\n"
      "ds_read_b64_tr_b16 a[228:229], v9 offset:16384\n"
      "ds_read_b64_tr_b16 a[230:231], v10 offset:16384\n"
      "v_mfma_f32_16x16x32_bf16 v[52:55], a[196:199], v[192:195], v[112:115]\n"
      "ds_read_b64_tr_b16 a[232:233], v7 offset:16896\n"
      "ds_read_b64_tr_b16 a[234:235], v8 offset:16896\n"
      "v_mfma_f32_16x16x32_bf16 v[60:63], a[196:199], v[208:211], v[116:119]\n"
      "buffer_load_dwordx4 v4, s[24:27], s63 offen offset:3072 nt lds\n"
      "v_mfma_f32_16x16x32_bf16 v[48:51], a[200:203], v[196:199], v[48:51]\n"
      "ds_read_b64_tr_b16 a[236:237], v9 offset:16896\n"
      "s_add_u32 m0, s40, 0x3000\n"
      "v_mfma_f32_16x16x32_bf16 v[56:59], a[200:203], v[212:215], v[56:59]\n"
      "ds_read_b64_tr_b16 a[238:239], v10 offset:16896\n"
      "ds_read_b64_tr_b16 a[240:241], v7 offset:17408\n"
      "v_mfma_f32_16x16x32_bf16 v[52:55], a[204:207], v[196:199], v[52:55]\n"
      "v_exp_f32 v24, v44\n"
      "v_mfma_f32_16x16x32_bf16 v[60:63], a[204:207], v[212:215], v[60:63]\n"
      "ds_read_b64_tr_b16 a[242:243], v8 offset:17408\n"
      "ds_read_b64_tr_b16 a[244:245], v9 offset:17408\n"
      "s_nop 7\n"
      "v_mfma_f32_16x16x32_bf16 v[48:51], a[208:211], v[200:203], v[48:51]\n"
      "ds_read_b64_tr_b16 a[246:247], v10 offset:17408\n"
      "ds_read_b64_tr_b16 a[248:249], v7 offset:17920\n"
      "v_mfma_f32_16x16x32_bf16 v[56:59], a[208:211], v[216:219], v[56:59]\n"
      "v_exp_f32 v25, v45\n"
      "v_mfma_f32_16x16x32_bf16 v[52:55], a[212:215], v[200:203], v[52:55]\n"
      "ds_read_b64_tr_b16 a[250:251], v8 offset:17920\n"
      "v_cvt_pk_bf16_f32 v85, v22, v23\n"
      "v_mfma_f32_16x16x32_bf16 v[60:63], a[212:215], v[216:219], v[60:63]\n"
      "ds_read_b64_tr_b16 a[252:253], v9 offset:17920\n"
      "ds_read_b64_tr_b16 a[254:255], v10 offset:17920\n"
      "v_mfma_f32_16x16x32_bf16 v[48:51], a[216:219], v[204:207], v[48:51]\n"
      "buffer_load_dwordx4 v1, s[24:27], s64 offen offset:0 nt lds\n"
      "v_mfma_f32_16x16x32_bf16 v[56:59], a[216:219], v[220:223], v[56:59]\n"
      "v_exp_f32 v156, v46\n"
      "v_mfma_f32_16x16x32_bf16 v[52:55], a[220:223], v[204:207], v[52:55]\n"
      "v_cvt_pk_bf16_f32 v86, v24, v25\n"
      "s_waitcnt lgkmcnt(8)\n"
      "v_mfma_f32_16x16x32_bf16 v[60:63], a[220:223], v[220:223], v[60:63]\n"
      "v_exp_f32 v157, v47\n"
      "s_nop 7\n"
      "v_mfma_f32_16x16x32_bf16 a[0:3], a[224:227], v[80:83], a[0:3]\n"
      "v_cvt_pk_bf16_f32 v87, v156, v157\n"
      "v_mfma_f32_16x16x32_bf16 a[4:7], a[228:231], v[80:83], a[4:7]\n"
      "v_exp_f32 v20, v48\n"
      "v_mfma_f32_16x16x32_bf16 v[132:135], v[152:155], v[84:87], v[132:135]\n"
      "v_exp_f32 v21, v49\n"
      "v_mfma_f32_16x16x32_bf16 a[8:11], a[232:235], v[80:83], a[8:11]\n"
      "buffer_load_dwordx4 v2, s[24:27], s64 offen offset:1024 nt lds\n"
      "v_mfma_f32_16x16x32_bf16 a[12:15], a[236:239], v[80:83], a[12:15]\n"
      "s_waitcnt lgkmcnt(0)\n"
      "v_mfma_f32_16x16x32_bf16 a[16:19], a[240:243], v[80:83], a[16:19]\n"
      "v_exp_f32 v22, v50\n"
      "v_mfma_f32_16x16x32_bf16 a[20:23], a[244:247], v[80:83], a[20:23]\n"
      "v_exp_f32 v23, v51\n"
      "v_mfma_f32_16x16x32_bf16 a[24:27], a[248:251], v[80:83], a[24:27]\n"
      "v_exp_f32 v24, v52\n"
      "v_mfma_f32_16x16x32_bf16 a[28:31], a[252:255], v[80:83], a[28:31]\n"
      "v_exp_f32 v25, v53\n"
      "s_nop 7\n"
      "v_mfma_f32_16x16x32_bf16 a[32:35], a[224:227], v[84:87], a[32:35]\n"
      "buffer_load_dwordx4 v3, s[24:27], s64 offen offset:2048 nt lds\n"
      "v_mfma_f32_16x16x32_bf16 a[36:39], a[228:231], v[84:87], a[36:39]\n"
      "v_cvt_pk_bf16_f32 v88, v20, v21\n"
      "v_cvt_pk_bf16_f32 v89, v22, v23\n"
      "v_mfma_f32_16x16x32_bf16 a[40:43], a[232:235], v[84:87], a[40:43]\n"
      "v_exp_f32 v156, v54\n"
      "v_mfma_f32_16x16x32_bf16 a[44:47], a[236:239], v[84:87], a[44:47]\n"
      "v_exp_f32 v157, v55\n"
      "v_mfma_f32_16x16x32_bf16 a[48:51], a[240:243], v[84:87], a[48:51]\n"
      "v_exp_f32 v20, v56\n"
      "v_mfma_f32_16x16x32_bf16 a[52:55], a[244:247], v[84:87], a[52:55]\n"
      "v_exp_f32 v21, v57\n"
      "v_mfma_f32_16x16x32_bf16 a[56:59], a[248:251], v[84:87], a[56:59]\n"
      "buffer_load_dwordx4 v4, s[24:27], s64 offen offset:3072 nt lds\n"
      "v_mfma_f32_16x16x32_bf16 a[60:63], a[252:255], v[84:87], a[60:63]\n"
      "s_add_u32 m0, s40, 0x4000\n"
      "s_add_u32 s63, s63, 0x8000\n"
      "s_nop 7\n"
      "v_mfma_f32_16x16x32_bf16 v[64:67], a[192:195], v[224:227], v[120:123]\n"
      "v_exp_f32 v22, v58\n"
      "v_mfma_f32_16x16x32_bf16 v[72:75], a[192:195], v[240:243], v[124:127]\n"
      "v_exp_f32 v23, v59\n"
      "v_mfma_f32_16x16x32_bf16 v[68:71], a[196:199], v[224:227], v[120:123]\n"
      "v_cvt_pk_bf16_f32 v90, v24, v25\n"
      "v_cvt_pk_bf16_f32 v91, v156, v157\n"
      "v_mfma_f32_16x16x32_bf16 v[76:79], a[196:199], v[240:243], v[124:127]\n"
      "v_exp_f32 v24, v60\n"
      "v_mfma_f32_16x16x32_bf16 v[136:139], v[152:155], v[88:91], v[136:139]\n"
      "v_exp_f32 v25, v61\n"
      "v_mfma_f32_16x16x32_bf16 v[64:67], a[200:203], v[228:231], v[64:67]\n"
      "v_exp_f32 v156, v62\n"
      "v_mfma_f32_16x16x32_bf16 v[72:75], a[200:203], v[244:247], v[72:75]\n"
      "v_exp_f32 v157, v63\n"
      "v_mfma_f32_16x16x32_bf16 v[68:71], a[204:207], v[228:231], v[68:71]\n"
      "v_cvt_pk_bf16_f32 v92, v20, v21\n"
      "v_cvt_pk_bf16_f32 v93, v22, v23\n"
      "v_mfma_f32_16x16x32_bf16 v[76:79], a[204:207], v[244:247], v[76:79]\n"
      "buffer_load_dwordx4 v1, s[20:23], s63 offen offset:0 nt lds\n"
      "s_nop 7\n"
      "v_mfma_f32_16x16x32_bf16 v[64:67], a[208:211], v[232:235], v[64:67]\n"
      "v_cvt_pk_bf16_f32 v94, v24, v25\n"
      "v_cvt_pk_bf16_f32 v95, v156, v157\n"
      "v_mfma_f32_16x16x32_bf16 v[72:75], a[208:211], v[248:251], v[72:75]\n"
      "v_add_u32 v30, s40, v14\n"
      "v_add_u32 v31, s41, v14\n"
      "v_mfma_f32_16x16x32_bf16 v[140:143], v[152:155], v[92:95], v[140:143]\n"
      "v_add_u32 v158, 6144, v31\n"
      "v_xor_b32 v26, 0x8000, v30\n"
      "v_mfma_f32_16x16x32_bf16 v[68:71], a[212:215], v[232:235], v[68:71]\n"
      "v_xor_b32 v27, 0x10000, v30\n"
      "v_xor_b32 v28, 0x18000, v30\n"
      "v_mfma_f32_16x16x32_bf16 v[76:79], a[212:215], v[248:251], v[76:79]\n"
      "s_waitcnt vmcnt(9)\n"
      "v_mfma_f32_16x16x32_bf16 v[64:67], a[216:219], v[236:239], v[64:67]\n"
      "ds_read_b128 a[192:195], v5 offset:0\n"
      "ds_read_b128 a[196:199], v6 offset:0\n"
      "v_mfma_f32_16x16x32_bf16 v[72:75], a[216:219], v[252:255], v[72:75]\n"
      "buffer_load_dwordx4 v2, s[20:23], s63 offen offset:1024 nt lds\n"
      "v_mfma_f32_16x16x32_bf16 v[68:71], a[220:223], v[236:239], v[68:71]\n"
      "ds_read_b128 a[200:203], v5 offset:512\n"
      "ds_read_b128 a[204:207], v6 offset:512\n"
      "v_mfma_f32_16x16x32_bf16 v[76:79], a[220:223], v[252:255], v[76:79]\n"
      "ds_read_b128 a[208:211], v5 offset:1024\n"
      "ds_read_b128 a[212:215], v6 offset:1024\n"
      "v_mfma_f32_16x16x32_bf16 a[64:67], a[224:227], v[88:91], a[64:67]\n"
      "ds_read_b128 a[216:219], v5 offset:1536\n"
      "ds_read_b128 a[220:223], v6 offset:1536\n"
      "v_mfma_f32_16x16x32_bf16 a[68:71], a[228:231], v[88:91], a[68:71]\n"
      "v_exp_f32 v20, v64\n"
      "v_mfma_f32_16x16x32_bf16 a[72:75], a[232:235], v[88:91], a[72:75]\n"
      "v_exp_f32 v21, v65\n"
      "v_mfma_f32_16x16x32_bf16 a[76:79], a[236:239], v[88:91], a[76:79]\n"
      "buffer_load_dwordx4 v3, s[20:23], s63 offen offset:2048 nt lds\n"
      "v_mfma_f32_16x16x32_bf16 a[80:83], a[240:243], v[88:91], a[80:83]\n"
      "v_exp_f32 v22, v66\n"
      "v_mfma_f32_16x16x32_bf16 a[84:87], a[244:247], v[88:91], a[84:87]\n"
      "v_exp_f32 v23, v67\n"
      "v_mfma_f32_16x16x32_bf16 a[88:91], a[248:251], v[88:91], a[88:91]\n"
      "v_cvt_pk_bf16_f32 v96, v20, v21\n"
      "v_cvt_pk_bf16_f32 v97, v22, v23\n"
      "v_mfma_f32_16x16x32_bf16 a[92:95], a[252:255], v[88:91], a[92:95]\n"
      "v_exp_f32 v24, v68\n"
      "v_mfma_f32_16x16x32_bf16 a[96:99], a[224:227], v[92:95], a[96:99]\n"
      "v_exp_f32 v25, v69\n"
      "v_mfma_f32_16x16x32_bf16 a[100:103], a[228:231], v[92:95], a[100:103]\n"
      "buffer_load_dwordx4 v4, s[20:23], s63 offen offset:3072 nt lds\n"
      "v_mfma_f32_16x16x32_bf16 a[104:107], a[232:235], v[92:95], a[104:107]\n"
      "v_exp_f32 v156, v70\n"
      "v_mfma_f32_16x16x32_bf16 a[108:111], a[236:239], v[92:95], a[108:111]\n"
      "s_add_u32 m0, s40, 0x5000\n"
      "s_add_u32 s64, s64, 0x8000\n"
      "v_mfma_f32_16x16x32_bf16 a[112:115], a[240:243], v[92:95], a[112:115]\n"
      "v_exp_f32 v157, v71\n"
      "v_mfma_f32_16x16x32_bf16 a[116:119], a[244:247], v[92:95], a[116:119]\n"
      "v_cvt_pk_bf16_f32 v98, v24, v25\n"
      "v_cvt_pk_bf16_f32 v99, v156, v157\n"
      "v_mfma_f32_16x16x32_bf16 a[120:123], a[248:251], v[92:95], a[120:123]\n"
      "v_xor_b32 v29, 0x6000, v31\n"
      "s_waitcnt lgkmcnt(0)\n"
      "v_mfma_f32_16x16x32_bf16 v[144:147], v[152:155], v[96:99], v[144:147]\n"
      "v_exp_f32 v20, v72\n"
      "v_mfma_f32_16x16x32_bf16 a[124:127], a[252:255], v[92:95], a[124:127]\n"
      "v_exp_f32 v21, v73\n"
      "v_mfma_f32_16x16x32_bf16 v[32:35], a[192:195], v[160:163], v[104:107]\n"
      "v_exp_f32 v22, v74\n"
      "v_mfma_f32_16x16x32_bf16 v[40:43], a[192:195], v[176:179], v[108:111]\n"
      "v_exp_f32 v23, v75\n"
      "v_mfma_f32_16x16x32_bf16 v[36:39], a[196:199], v[160:163], v[104:107]\n"
      "buffer_load_dwordx4 v1, s[20:23], s64 offen offset:0 nt lds\n"
      "v_mfma_f32_16x16x32_bf16 v[44:47], a[196:199], v[176:179], v[108:111]\n"
      "v_exp_f32 v24, v76\n"
      "v_mfma_f32_16x16x32_bf16 v[32:35], a[200:203], v[164:167], v[32:35]\n"
      "v_exp_f32 v25, v77\n"
      "v_mfma_f32_16x16x32_bf16 v[40:43], a[200:203], v[180:183], v[40:43]\n"
      "v_exp_f32 v156, v78\n"
      "v_mfma_f32_16x16x32_bf16 v[36:39], a[204:207], v[164:167], v[36:39]\n"
      "v_exp_f32 v157, v79\n"
      "v_mfma_f32_16x16x32_bf16 v[44:47], a[204:207], v[180:183], v[44:47]\n"
      "v_cvt_pk_bf16_f32 v100, v20, v21\n"
      "v_cvt_pk_bf16_f32 v101, v22, v23\n"
      "v_mfma_f32_16x16x32_bf16 v[32:35], a[208:211], v[168:171], v[32:35]\n"
      "buffer_load_dwordx4 v2, s[20:23], s64 offen offset:1024 nt lds\n"
      "v_mfma_f32_16x16x32_bf16 v[40:43], a[208:211], v[184:187], v[40:43]\n"
      "v_cvt_pk_bf16_f32 v102, v24, v25\n"
      "v_cvt_pk_bf16_f32 v103, v156, v157\n"
      "v_mfma_f32_16x16x32_bf16 v[36:39], a[212:215], v[168:171], v[36:39]\n"
      "s_nop 0\n"
      "v_mfma_f32_16x16x32_bf16 v[148:151], v[152:155], v[100:103], v[148:151]\n"
      "v_mfma_f32_16x16x32_bf16 v[44:47], a[212:215], v[184:187], v[44:47]\n"
      "v_mfma_f32_16x16x32_bf16 v[32:35], a[216:219], v[172:175], v[32:35]\n"
      "buffer_load_dwordx4 v3, s[20:23], s64 offen offset:2048 nt lds\n"
      "v_mfma_f32_16x16x32_bf16 v[40:43], a[216:219], v[188:191], v[40:43]\n"
      "v_mfma_f32_16x16x32_bf16 v[36:39], a[220:223], v[172:175], v[36:39]\n"
      "v_mfma_f32_16x16x32_bf16 v[44:47], a[220:223], v[188:191], v[44:47]\n"
      "v_mfma_f32_16x16x32_bf16 a[128:131], a[224:227], v[96:99], a[128:131]\n"
      "buffer_load_dwordx4 v4, s[20:23], s64 offen offset:3072 nt lds\n"
      "v_mfma_f32_16x16x32_bf16 a[132:135], a[228:231], v[96:99], a[132:135]\n"
      "s_nop 0\n"
      "v_exp_f32 v20, v32\n"
      "v_mfma_f32_16x16x32_bf16 a[136:139], a[232:235], v[96:99], a[136:139]\n"
      "v_exp_f32 v21, v33\n"
      "v_mfma_f32_16x16x32_bf16 a[140:143], a[236:239], v[96:99], a[140:143]\n"
      "v_exp_f32 v22, v34\n"
      "v_mfma_f32_16x16x32_bf16 a[144:147], a[240:243], v[96:99], a[144:147]\n"
      "v_exp_f32 v23, v35\n"
      "v_mfma_f32_16x16x32_bf16 a[148:151], a[244:247], v[96:99], a[148:151]\n"
      "v_cvt_pk_bf16_f32 v80, v20, v21\n"
      "s_add_u32 m0, s40, 0x6000\n"
      "v_mfma_f32_16x16x32_bf16 a[152:155], a[248:251], v[96:99], a[152:155]\n"
      "v_exp_f32 v24, v36\n"
      "v_mfma_f32_16x16x32_bf16 a[156:159], a[252:255], v[96:99], a[156:159]\n"
      "v_exp_f32 v25, v37\n"
      "v_mfma_f32_16x16x32_bf16 a[160:163], a[224:227], v[100:103], a[160:163]\n"
      "v_exp_f32 v156, v38\n"
      "v_mfma_f32_16x16x32_bf16 a[164:167], a[228:231], v[100:103], a[164:167]\n"
      "v_cvt_pk_bf16_f32 v81, v22, v23\n"
      "s_waitcnt vmcnt(8)\n"
      "v_mfma_f32_16x16x32_bf16 a[168:171], a[232:235], v[100:103], a[168:171]\n"
      "v_exp_f32 v157, v39\n"
      "v_mfma_f32_16x16x32_bf16 a[172:175], a[236:239], v[100:103], a[172:175]\n"
      "v_cvt_pk_bf16_f32 v82, v24, v25\n"
      "v_cvt_pk_bf16_f32 v83, v156, v157\n"
      "v_mfma_f32_16x16x32_bf16 a[176:179], a[240:243], v[100:103], a[176:179]\n"
      "v_exp_f32 v20, v40\n"
      "v_mfma_f32_16x16x32_bf16 v[128:131], v[152:155], v[80:83], v[128:131]\n"
      "v_exp_f32 v21, v41\n"
      "v_mfma_f32_16x16x32_bf16 a[180:183], a[244:247], v[100:103], a[180:183]\n"
      "buffer_load_dwordx4 v1, s[24:27], s63 offen offset:0 nt lds\n"
      "v_mfma_f32_16x16x32_bf16 a[184:187], a[248:251], v[100:103], a[184:187]\n"
      "v_exp_f32 v22, v42\n"
      "v_mfma_f32_16x16x32_bf16 a[188:191], a[252:255], v[100:103], a[188:191]\n"
      "ds_read_b64_tr_b16 a[224:225], v7 offset:0\n"
      "ds_read_b64_tr_b16 a[226:227], v8 offset:0\n"
      "v_mfma_f32_16x16x32_bf16 v[48:51], a[192:195], v[192:195], v[112:115]\n"
      "v_exp_f32 v23, v43\n"
      "v_mfma_f32_16x16x32_bf16 v[56:59], a[192:195], v[208:211], v[116:119]\n"
      "ds_read_b64_tr_b16 a[228:229], v9 offset:0\n"
      "ds_read_b64_tr_b16 a[230:231], v10 offset:0\n"
      "v_mfma_f32_16x16x32_bf16 v[52:55], a[196:199], v[192:195], v[112:115]\n"
      "ds_read_b64_tr_b16 a[232:233], v7 offset:512\n"
      "ds_read_b64_tr_b16 a[234:235], v8 offset:512\n"
      "v_mfma_f32_16x16x32_bf16 v[60:63], a[196:199], v[208:211], v[116:119]\n"
      "buffer_load_dwordx4 v2, s[24:27], s63 offen offset:1024 nt lds\n"
      "v_mfma_f32_16x16x32_bf16 v[48:51], a[200:203], v[196:199], v[48:51]\n"
      "ds_read_b64_tr_b16 a[236:237], v9 offset:512\n"
      "v_cvt_pk_bf16_f32 v84, v20, v21\n"
      "v_mfma_f32_16x16x32_bf16 v[56:59], a[200:203], v[212:215], v[56:59]\n"
      "ds_read_b64_tr_b16 a[238:239], v10 offset:512\n"
      "ds_read_b64_tr_b16 a[240:241], v7 offset:1024\n"
      "v_mfma_f32_16x16x32_bf16 v[52:55], a[204:207], v[196:199], v[52:55]\n"
      "v_exp_f32 v24, v44\n"
      "v_mfma_f32_16x16x32_bf16 v[60:63], a[204:207], v[212:215], v[60:63]\n"
      "ds_read_b64_tr_b16 a[242:243], v8 offset:1024\n"
      "ds_read_b64_tr_b16 a[244:245], v9 offset:1024\n"
      "v_mfma_f32_16x16x32_bf16 v[48:51], a[208:211], v[200:203], v[48:51]\n"
      "ds_read_b64_tr_b16 a[246:247], v10 offset:1024\n"
      "ds_read_b64_tr_b16 a[248:249], v7 offset:1536\n"
      "v_mfma_f32_16x16x32_bf16 v[56:59], a[208:211], v[216:219], v[56:59]\n"
      "buffer_load_dwordx4 v3, s[24:27], s63 offen offset:2048 nt lds\n"
      "v_mfma_f32_16x16x32_bf16 v[52:55], a[212:215], v[200:203], v[52:55]\n"
      "v_exp_f32 v25, v45\n"
      "v_mfma_f32_16x16x32_bf16 v[60:63], a[212:215], v[216:219], v[60:63]\n"
      "ds_read_b64_tr_b16 a[250:251], v8 offset:1536\n"
      "v_cvt_pk_bf16_f32 v85, v22, v23\n"
      "v_mfma_f32_16x16x32_bf16 v[48:51], a[216:219], v[204:207], v[48:51]\n"
      "ds_read_b64_tr_b16 a[252:253], v9 offset:1536\n"
      "ds_read_b64_tr_b16 a[254:255], v10 offset:1536\n"
      "v_mfma_f32_16x16x32_bf16 v[56:59], a[216:219], v[220:223], v[56:59]\n"
      "v_exp_f32 v156, v46\n"
      "v_mfma_f32_16x16x32_bf16 v[52:55], a[220:223], v[204:207], v[52:55]\n"
      "v_cvt_pk_bf16_f32 v86, v24, v25\n"
      "s_waitcnt lgkmcnt(8)\n"
      "v_mfma_f32_16x16x32_bf16 v[60:63], a[220:223], v[220:223], v[60:63]\n"
      "buffer_load_dwordx4 v4, s[24:27], s63 offen offset:3072 nt lds\n"
      "v_mfma_f32_16x16x32_bf16 a[0:3], a[224:227], v[80:83], a[0:3]\n"
      "v_exp_f32 v157, v47\n"
      "v_mfma_f32_16x16x32_bf16 a[4:7], a[228:231], v[80:83], a[4:7]\n"
      "s_add_u32 m0, s40, 0x7000\n"
      "v_cvt_pk_bf16_f32 v87, v156, v157\n"
      "v_exp_f32 v20, v48\n"
      "v_mfma_f32_16x16x32_bf16 a[8:11], a[232:235], v[80:83], a[8:11]\n"
      "v_exp_f32 v21, v49\n"
      "v_mfma_f32_16x16x32_bf16 v[132:135], v[152:155], v[84:87], v[132:135]\n"
      "v_exp_f32 v22, v50\n"
      "v_mfma_f32_16x16x32_bf16 a[12:15], a[236:239], v[80:83], a[12:15]\n"
      "s_waitcnt lgkmcnt(0)\n"
      "v_mfma_f32_16x16x32_bf16 a[16:19], a[240:243], v[80:83], a[16:19]\n"
      "v_exp_f32 v23, v51\n"
      "v_mfma_f32_16x16x32_bf16 a[20:23], a[244:247], v[80:83], a[20:23]\n"
      "v_exp_f32 v24, v52\n"
      "v_mfma_f32_16x16x32_bf16 a[24:27], a[248:251], v[80:83], a[24:27]\n"
      "ds_write_b128 v30, a[8:11] offset:0\n"
      "v_mfma_f32_16x16x32_bf16 a[28:31], a[252:255], v[80:83], a[28:31]\n"
      "v_exp_f32 v25, v53\n"
      "v_mfma_f32_16x16x32_bf16 a[32:35], a[224:227], v[84:87], a[32:35]\n"
      "buffer_load_dwordx4 v1, s[24:27], s64 offen offset:0 nt lds\n"
      "v_mfma_f32_16x16x32_bf16 a[36:39], a[228:231], v[84:87], a[36:39]\n"
      "v_cvt_pk_bf16_f32 v88, v20, v21\n"
      "v_cvt_pk_bf16_f32 v89, v22, v23\n"
      "v_mfma_f32_16x16x32_bf16 a[40:43], a[232:235], v[84:87], a[40:43]\n"
      "ds_write_b128 v30, a[12:15] offset:1024\n"
      "v_mfma_f32_16x16x32_bf16 a[44:47], a[236:239], v[84:87], a[44:47]\n"
      "v_exp_f32 v156, v54\n"
      "v_mfma_f32_16x16x32_bf16 a[48:51], a[240:243], v[84:87], a[48:51]\n"
      "v_exp_f32 v157, v55\n"
      "v_mfma_f32_16x16x32_bf16 a[52:55], a[244:247], v[84:87], a[52:55]\n"
      "ds_write_b128 v30, a[16:19] offset:6144\n"
      "v_mfma_f32_16x16x32_bf16 a[56:59], a[248:251], v[84:87], a[56:59]\n"
      "buffer_load_dwordx4 v2, s[24:27], s64 offen offset:1024 nt lds\n"
      "v_mfma_f32_16x16x32_bf16 a[60:63], a[252:255], v[84:87], a[60:63]\n"
      "v_exp_f32 v20, v56\n"
      "v_mfma_f32_16x16x32_bf16 v[64:67], a[192:195], v[224:227], v[120:123]\n"
      "ds_write_b128 v30, a[20:23] offset:7168\n"
      "v_mfma_f32_16x16x32_bf16 v[72:75], a[192:195], v[240:243], v[124:127]\n"
      "v_exp_f32 v21, v57\n"
      "v_mfma_f32_16x16x32_bf16 v[68:71], a[196:199], v[224:227], v[120:123]\n"
      "v_cvt_pk_bf16_f32 v90, v24, v25\n"
      "v_cvt_pk_bf16_f32 v91, v156, v157\n"
      "v_mfma_f32_16x16x32_bf16 v[76:79], a[196:199], v[240:243], v[124:127]\n"
      "ds_write_b128 v30, a[24:27] offset:12288\n"
      "v_mfma_f32_16x16x32_bf16 v[136:139], v[152:155], v[88:91], v[136:139]\n"
      "v_exp_f32 v22, v58\n"
      "v_mfma_f32_16x16x32_bf16 v[64:67], a[200:203], v[228:231], v[64:67]\n"
      "buffer_load_dwordx4 v3, s[24:27], s64 offen offset:2048 nt lds\n"
      "v_mfma_f32_16x16x32_bf16 v[72:75], a[200:203], v[244:247], v[72:75]\n"
      "v_exp_f32 v23, v59\n"
      "v_mfma_f32_16x16x32_bf16 v[68:71], a[204:207], v[228:231], v[68:71]\n"
      "v_exp_f32 v24, v60\n"
      "v_mfma_f32_16x16x32_bf16 v[76:79], a[204:207], v[244:247], v[76:79]\n"
      "ds_write_b128 v30, a[28:31] offset:13312\n"
      "v_mfma_f32_16x16x32_bf16 v[64:67], a[208:211], v[232:235], v[64:67]\n"
      "v_exp_f32 v25, v61\n"
      "v_mfma_f32_16x16x32_bf16 v[72:75], a[208:211], v[248:251], v[72:75]\n"
      "v_exp_f32 v156, v62\n"
      "v_mfma_f32_16x16x32_bf16 v[68:71], a[212:215], v[232:235], v[68:71]\n"
      "buffer_load_dwordx4 v4, s[24:27], s64 offen offset:3072 nt lds\n"
      "v_mfma_f32_16x16x32_bf16 v[76:79], a[212:215], v[248:251], v[76:79]\n"
      "ds_write_b128 v30, a[40:43] offset:2048\n"
      "v_cvt_pk_bf16_f32 v92, v20, v21\n"
      "v_mfma_f32_16x16x32_bf16 v[64:67], a[216:219], v[236:239], v[64:67]\n"
      "v_cvt_pk_bf16_f32 v93, v22, v23\n"
      "s_waitcnt vmcnt(8)\n"
      "v_mfma_f32_16x16x32_bf16 v[72:75], a[216:219], v[252:255], v[72:75]\n"
      "v_exp_f32 v157, v63\n"
      "v_mfma_f32_16x16x32_bf16 v[68:71], a[220:223], v[236:239], v[68:71]\n"
      "ds_write_b128 v30, a[44:47] offset:3072\n"
      "v_mfma_f32_16x16x32_bf16 v[76:79], a[220:223], v[252:255], v[76:79]\n"
      "ds_read_b128 a[192:195], v5 offset:16384\n"
      "v_cvt_pk_bf16_f32 v94, v24, v25\n"
      "v_mfma_f32_16x16x32_bf16 a[64:67], a[224:227], v[88:91], a[64:67]\n"
      "ds_read_b128 a[196:199], v6 offset:16384\n"
      "v_cvt_pk_bf16_f32 v95, v156, v157\n"
      "v_mfma_f32_16x16x32_bf16 a[68:71], a[228:231], v[88:91], a[68:71]\n"
      "v_exp_f32 v20, v64\n"
      "v_mfma_f32_16x16x32_bf16 v[140:143], v[152:155], v[92:95], v[140:143]\n"
      "ds_write_b128 v30, a[48:51] offset:8192\n"
      "v_mfma_f32_16x16x32_bf16 a[72:75], a[232:235], v[88:91], a[72:75]\n"
      "v_exp_f32 v21, v65\n"
      "v_mfma_f32_16x16x32_bf16 a[76:79], a[236:239], v[88:91], a[76:79]\n"
      "v_exp_f32 v22, v66\n"
      "v_mfma_f32_16x16x32_bf16 a[80:83], a[240:243], v[88:91], a[80:83]\n"
      "v_exp_f32 v23, v67\n"
      "v_mfma_f32_16x16x32_bf16 a[84:87], a[244:247], v[88:91], a[84:87]\n"
      "ds_write_b128 v30, a[52:55] offset:9216\n"
      "v_mfma_f32_16x16x32_bf16 a[88:91], a[248:251], v[88:91], a[88:91]\n"
      "ds_read_b128 a[200:203], v5 offset:16896\n"
      "v_cvt_pk_bf16_f32 v96, v20, v21\n"
      "v_mfma_f32_16x16x32_bf16 a[92:95], a[252:255], v[88:91], a[92:95]\n"
      "v_exp_f32 v24, v68\n"
      "v_mfma_f32_16x16x32_bf16 a[96:99], a[224:227], v[92:95], a[96:99]\n"
      "ds_write_b128 v30, a[56:59] offset:14336\n"
      "v_mfma_f32_16x16x32_bf16 a[100:103], a[228:231], v[92:95], a[100:103]\n"
      "ds_read_b128 a[204:207], v6 offset:16896\n"
      "ds_read_b128 a[208:211], v5 offset:17408\n"
      "v_mfma_f32_16x16x32_bf16 a[104:107], a[232:235], v[92:95], a[104:107]\n"
      "v_exp_f32 v25, v69\n"
      "v_mfma_f32_16x16x32_bf16 a[108:111], a[236:239], v[92:95], a[108:111]\n"
      "ds_write_b128 v30, a[60:63] offset:15360\n"
      "v_mfma_f32_16x16x32_bf16 a[112:115], a[240:243], v[92:95], a[112:115]\n"
      "ds_read_b128 a[212:215], v6 offset:17408\n"
      "v_cvt_pk_bf16_f32 v97, v22, v23\n"
      "v_mfma_f32_16x16x32_bf16 a[116:119], a[244:247], v[92:95], a[116:119]\n"
      "v_exp_f32 v156, v70\n"
      "v_mfma_f32_16x16x32_bf16 a[120:123], a[248:251], v[92:95], a[120:123]\n"
      "v_exp_f32 v157, v71\n"
      "v_mfma_f32_16x16x32_bf16 a[124:127], a[252:255], v[92:95], a[124:127]\n"
      "ds_write_b128 v30, a[72:75] offset:4096\n"
      "s_waitcnt lgkmcnt(2)\n"
      "v_mfma_f32_16x16x32_bf16 v[32:35], a[192:195], v[160:163], v[104:107]\n"
      "ds_read_b128 a[216:219], v5 offset:17920\n"
      "v_cvt_pk_bf16_f32 v98, v24, v25\n"
      "v_mfma_f32_16x16x32_bf16 v[40:43], a[192:195], v[176:179], v[108:111]\n"
      "ds_read_b128 a[220:223], v6 offset:17920\n"
      "v_cvt_pk_bf16_f32 v99, v156, v157\n"
      "v_mfma_f32_16x16x32_bf16 v[36:39], a[196:199], v[160:163], v[104:107]\n"
      "ds_write_b128 v30, a[76:79] offset:5120\n"
      "v_mfma_f32_16x16x32_bf16 v[144:147], v[152:155], v[96:99], v[144:147]\n"
      "v_exp_f32 v20, v72\n"
      "v_mfma_f32_16x16x32_bf16 v[44:47], a[196:199], v[176:179], v[108:111]\n"
      "v_exp_f32 v21, v73\n"
      "v_mfma_f32_16x16x32_bf16 v[32:35], a[200:203], v[164:167], v[32:35]\n"
      "s_waitcnt lgkmcnt(1)\n"
      "v_mfma_f32_16x16x32_bf16 v[40:43], a[200:203], v[180:183], v[40:43]\n"
      "v_exp_f32 v22, v74\n"
      "v_mfma_f32_16x16x32_bf16 v[36:39], a[204:207], v[164:167], v[36:39]\n"
      "ds_write_b128 v30, a[80:83] offset:10240\n"
      "v_mfma_f32_16x16x32_bf16 v[44:47], a[204:207], v[180:183], v[44:47]\n"
      "v_exp_f32 v23, v75\n"
      "v_mfma_f32_16x16x32_bf16 v[32:35], a[208:211], v[168:171], v[32:35]\n"
      "v_exp_f32 v24, v76\n"
      "v_mfma_f32_16x16x32_bf16 v[40:43], a[208:211], v[184:187], v[40:43]\n"
      "ds_write_b128 v30, a[84:87] offset:11264\n"
      "v_mfma_f32_16x16x32_bf16 v[36:39], a[212:215], v[168:171], v[36:39]\n"
      "v_exp_f32 v25, v77\n"
      "v_mfma_f32_16x16x32_bf16 v[44:47], a[212:215], v[184:187], v[44:47]\n"
      "v_cvt_pk_bf16_f32 v100, v20, v21\n"
      "v_cvt_pk_bf16_f32 v101, v22, v23\n"
      "v_mfma_f32_16x16x32_bf16 v[32:35], a[216:219], v[172:175], v[32:35]\n"
      "v_exp_f32 v156, v78\n"
      "v_mfma_f32_16x16x32_bf16 v[40:43], a[216:219], v[188:191], v[40:43]\n"
      "ds_write_b128 v31, a[88:91] offset:0\n"
      "v_mfma_f32_16x16x32_bf16 v[36:39], a[220:223], v[172:175], v[36:39]\n"
      "v_exp_f32 v157, v79\n"
      "v_mfma_f32_16x16x32_bf16 v[44:47], a[220:223], v[188:191], v[44:47]\n"
      "v_cvt_pk_bf16_f32 v102, v24, v25\n"
      "v_cvt_pk_bf16_f32 v103, v156, v157\n"
      "v_mfma_f32_16x16x32_bf16 a[128:131], a[224:227], v[96:99], a[128:131]\n"
      "ds_write_b128 v31, a[92:95] offset:1024\n"
      "v_mfma_f32_16x16x32_bf16 v[148:151], v[152:155], v[100:103], v[148:151]\n"
      "v_mfma_f32_16x16x32_bf16 a[132:135], a[228:231], v[96:99], a[132:135]\n"
      "v_exp_f32 v20, v32\n"
      "v_mfma_f32_16x16x32_bf16 a[136:139], a[232:235], v[96:99], a[136:139]\n"
      "v_exp_f32 v21, v33\n"
      "v_mfma_f32_16x16x32_bf16 a[140:143], a[236:239], v[96:99], a[140:143]\n"
      "v_exp_f32 v22, v34\n"
      "v_mfma_f32_16x16x32_bf16 a[144:147], a[240:243], v[96:99], a[144:147]\n"
      "v_exp_f32 v23, v35\n"
      "v_mfma_f32_16x16x32_bf16 a[148:151], a[244:247], v[96:99], a[148:151]\n"
      "v_exp_f32 v24, v36\n"
      "v_mfma_f32_16x16x32_bf16 a[152:155], a[248:251], v[96:99], a[152:155]\n"
      "v_exp_f32 v25, v37\n"
      "v_mfma_f32_16x16x32_bf16 a[156:159], a[252:255], v[96:99], a[156:159]\n"
      "v_exp_f32 v156, v38\n"
      "v_mfma_f32_16x16x32_bf16 a[160:163], a[224:227], v[100:103], a[160:163]\n"
      "v_cvt_pk_bf16_f32 v80, v20, v21\n"
      "v_cvt_pk_bf16_f32 v81, v22, v23\n"
      "v_mfma_f32_16x16x32_bf16 a[164:167], a[228:231], v[100:103], a[164:167]\n"
      "s_waitcnt lgkmcnt(0)\n"
      "s_barrier\n"
      "v_mfma_f32_16x16x32_bf16 a[168:171], a[232:235], v[100:103], a[168:171]\n"
      "ds_read_b128 v[160:163], v26 offset:0\n"
      "ds_read_b128 v[164:167], v26 offset:1024\n"
      "ds_read_b128 v[184:187], v27 offset:6144\n"
      "v_mfma_f32_16x16x32_bf16 a[172:175], a[236:239], v[100:103], a[172:175]\n"
      "ds_read_b128 v[188:191], v27 offset:7168\n"
      "ds_read_b128 v[104:107], v28 offset:12288\n"
      "v_mfma_f32_16x16x32_bf16 a[176:179], a[240:243], v[100:103], a[176:179]\n"
      "ds_read_b128 v[108:111], v28 offset:13312\n"
      "v_exp_f32 v157, v39\n"
      "v_mfma_f32_16x16x32_bf16 a[180:183], a[244:247], v[100:103], a[180:183]\n"
      "v_exp_f32 v20, v40\n"
      "v_mfma_f32_16x16x32_bf16 a[184:187], a[248:251], v[100:103], a[184:187]\n"
      "v_exp_f32 v21, v41\n"
      "v_mfma_f32_16x16x32_bf16 a[188:191], a[252:255], v[100:103], a[188:191]\n"
      "v_cvt_pk_bf16_f32 v82, v24, v25\n"
      "v_cvt_pk_bf16_f32 v83, v156, v157\n"
      "v_mfma_f32_16x16x32_bf16 v[48:51], a[192:195], v[192:195], v[112:115]\n"
      "s_waitcnt lgkmcnt(0)\n"
      "v_mfma_f32_16x16x32_bf16 v[128:131], v[152:155], v[80:83], v[128:131]\n"
      "v_exp_f32 v22, v42\n"
      "v_mfma_f32_16x16x32_bf16 v[56:59], a[192:195], v[208:211], v[116:119]\n"
      "v_add_f32 v160, v160, v184\n"
      "v_add_f32 v161, v161, v185\n"
      "v_mfma_f32_16x16x32_bf16 v[52:55], a[196:199], v[192:195], v[112:115]\n"
      "v_add_f32 v162, v162, v186\n"
      "v_add_f32 v163, v163, v187\n"
      "v_mfma_f32_16x16x32_bf16 v[60:63], a[196:199], v[208:211], v[116:119]\n"
      "v_exp_f32 v23, v43\n"
      "v_add_f32 v164, v164, v188\n"
      "v_add_f32 v165, v165, v189\n"
      "v_add_f32 v166, v166, v190\n"
      "v_add_f32 v167, v167, v191\n"
      "v_mfma_f32_16x16x32_bf16 v[48:51], a[200:203], v[196:199], v[48:51]\n"
      "v_add_f32 v160, v160, v104\n"
      "v_add_f32 v161, v161, v105\n"
      "v_add_f32 v162, v162, v106\n"
      "v_add_f32 v163, v163, v107\n"
      "v_mfma_f32_16x16x32_bf16 v[56:59], a[200:203], v[212:215], v[56:59]\n"
      "v_exp_f32 v24, v44\n"
      "v_add_f32 v164, v164, v108\n"
      "v_add_f32 v165, v165, v109\n"
      "v_add_f32 v166, v166, v110\n"
      "v_mfma_f32_16x16x32_bf16 v[52:55], a[204:207], v[196:199], v[52:55]\n"
      "v_cvt_pk_bf16_f32 v84, v20, v21\n"
      "v_exp_f32 v25, v45\n"
      "v_add_f32 v167, v167, v111\n"
      "v_mfma_f32_16x16x32_bf16 v[60:63], a[204:207], v[212:215], v[60:63]\n"
      "ds_read_b128 v[168:171], v26 offset:2048\n"
      "ds_read_b128 v[172:175], v26 offset:3072\n"
      "ds_read_b128 v[184:187], v27 offset:8192\n"
      "ds_read_b128 v[188:191], v27 offset:9216\n"
      "ds_read_b128 v[104:107], v28 offset:14336\n"
      "ds_read_b128 v[108:111], v28 offset:15360\n"
      "v_mfma_f32_16x16x32_bf16 v[48:51], a[208:211], v[200:203], v[48:51]\n"
      "buffer_load_dword v19, off, s[16:19], s49 sc1\n"
      "v_exp_f32 v156, v46\n"
      "v_mfma_f32_16x16x32_bf16 v[56:59], a[208:211], v[216:219], v[56:59]\n"
      "v_cvt_pk_bf16_f32 v85, v22, v23\n"
      "v_cvt_pk_bf16_f32 v86, v24, v25\n"
      "v_mfma_f32_16x16x32_bf16 v[52:55], a[212:215], v[200:203], v[52:55]\n"
      "v_exp_f32 v157, v47\n"
      "v_mfma_f32_16x16x32_bf16 v[60:63], a[212:215], v[216:219], v[60:63]\n"
      "s_waitcnt lgkmcnt(0)\n"
      "v_mfma_f32_16x16x32_bf16 v[48:51], a[216:219], v[204:207], v[48:51]\n"
      "ds_write_b128 v31, v[80:83] offset:2048\n"
      "v_add_f32 v168, v168, v184\n"
      "v_add_f32 v169, v169, v185\n"
      "v_add_f32 v170, v170, v186\n"
      "v_add_f32 v171, v171, v187\n"
      "v_mfma_f32_16x16x32_bf16 v[56:59], a[216:219], v[220:223], v[56:59]\n"
      "v_add_f32 v172, v172, v188\n"
      "v_add_f32 v173, v173, v189\n"
      "v_add_f32 v174, v174, v190\n"
      "v_add_f32 v175, v175, v191\n"
      "v_mfma_f32_16x16x32_bf16 v[52:55], a[220:223], v[204:207], v[52:55]\n"
      "v_cvt_pk_bf16_f32 v87, v156, v157\n"
      "v_add_f32 v168, v168, v104\n"
      "v_add_f32 v169, v169, v105\n"
      "v_add_f32 v170, v170, v106\n"
      "v_add_f32 v171, v171, v107\n"
      "v_mfma_f32_16x16x32_bf16 v[60:63], a[220:223], v[220:223], v[60:63]\n"
      "v_add_f32 v172, v172, v108\n"
      "v_add_f32 v173, v173, v109\n"
      "v_add_f32 v174, v174, v110\n"
      "v_mfma_f32_16x16x32_bf16 v[132:135], v[152:155], v[84:87], v[132:135]\n"
      "ds_write_b128 v31, v[84:87] offset:3072\n"
      "v_exp_f32 v20, v48\n"
      "v_exp_f32 v21, v49\n"
      "v_exp_f32 v22, v50\n"
      "v_exp_f32 v23, v51\n"
      "v_exp_f32 v24, v52\n"
      "v_add_f32 v175, v175, v111\n"
      "ds_read_b128 v[176:179], v26 offset:4096\n"
      "ds_read_b128 v[180:183], v26 offset:5120\n"
      "ds_read_b128 v[184:187], v27 offset:10240\n"
      "ds_read_b128 v[188:191], v27 offset:11264\n"
      "ds_read_b128 v[104:107], v29 offset:0\n"
      "ds_read_b128 v[108:111], v29 offset:1024\n"
      "v_exp_f32 v25, v53\n"
      "v_xor_b32 v192, 0x8000, v7\n"
      "v_xor_b32 v193, 0x8000, v8\n"
      "v_xor_b32 v194, 0x8000, v9\n"
      "v_xor_b32 v195, 0x8000, v10\n"
      "v_xor_b32 v196, 0x10000, v7\n"
      "v_xor_b32 v197, 0x10000, v8\n"
      "v_xor_b32 v198, 0x10000, v9\n"
      "v_exp_f32 v156, v54\n"
      "v_xor_b32 v199, 0x10000, v10\n"
      "v_xor_b32 v200, 0x18000, v7\n"
      "v_xor_b32 v201, 0x18000, v8\n"
      "v_xor_b32 v202, 0x18000, v9\n"
      "v_xor_b32 v203, 0x18000, v10\n"
      "v_xor_b32 v204, 0x2000, v31\n"
      "v_xor_b32 v205, 0x4000, v31\n"
      "v_cvt_pk_bf16_f32 v88, v20, v21\n"
      "v_exp_f32 v157, v55\n"
      "v_xor_b32 v206, 0x6000, v31\n"
      "v_cvt_pk_bf16_f32 v89, v22, v23\n"
      "v_cvt_pk_bf16_f32 v90, v24, v25\n"
      "v_exp_f32 v20, v56\n"
      "v_cvt_pk_bf16_f32 v91, v156, v157\n"
      "v_exp_f32 v21, v57\n"
      "v_exp_f32 v22, v58\n"
      "v_exp_f32 v23, v59\n"
      "s_waitcnt lgkmcnt(0)\n"
      "v_mfma_f32_16x16x32_bf16 v[136:139], v[152:155], v[88:91], v[136:139]\n"
      "v_exp_f32 v24, v60\n"
      "ds_write_b128 v31, v[88:91] offset:4096\n"
      "v_add_f32 v176, v176, v184\n"
      "v_add_f32 v177, v177, v185\n"
      "v_add_f32 v178, v178, v186\n"
      "v_add_f32 v179, v179, v187\n"
      "v_add_f32 v180, v180, v188\n"
      "v_add_f32 v181, v181, v189\n"
      "v_add_f32 v182, v182, v190\n"
      "v_add_f32 v183, v183, v191\n"
      "v_add_f32 v176, v176, v104\n"
      "v_add_f32 v177, v177, v105\n"
      "v_add_f32 v178, v178, v106\n"
      "v_add_f32 v179, v179, v107\n"
      "v_cndmask_b32 v16, v128, v132, s[56:57]\n"
      "v_cndmask_b32 v16, v16, v136, s[58:59]\n"
      "v_med3_f32 v18, v16, v15, s42\n"
      "v_cmp_neq_f32 vcc, v18, v16\n"
      "s_nop 0\n"
      "s_cmp_lg_u64 vcc, 0\n"
      "s_cselect_b32 s45, 1, 0\n"
      "v_mov_b32 v18, s45\n"
      "v_add_f32 v180, v180, v108\n"
      "ds_write2_b32 v158, v16, v18 offset0:0 offset1:1\n"
      "v_add_f32 v181, v181, v109\n"
      "v_add_f32 v182, v182, v110\n"
      "v_add_f32 v183, v183, v111\n"
      "v_exp_f32 v25, v61\n"
      "s_waitcnt vmcnt(1)\n"
      "s_waitcnt lgkmcnt(0)\n"
      "s_barrier\n"
      "ds_read_b64_tr_b16 a[224:225], v7 offset:16384\n"
      "ds_read_b64_tr_b16 a[226:227], v8 offset:16384\n"
      "ds_read_b64_tr_b16 a[228:229], v9 offset:16384\n"
      "ds_read_b64_tr_b16 a[230:231], v10 offset:16384\n"
      "ds_read_b128 a[8:11], v204 offset:2048\n"
      "ds_read_b64_tr_b16 a[20:21], v192 offset:16896\n"
      "ds_read_b64_tr_b16 a[22:23], v193 offset:16896\n"
      "ds_read_b64_tr_b16 a[24:25], v194 offset:16896\n"
      "ds_read_b64_tr_b16 a[26:27], v195 offset:16896\n"
      "ds_read_b128 a[40:43], v205 offset:2048\n"
      "ds_read_b64_tr_b16 a[52:53], v196 offset:17408\n"
      "ds_read_b64_tr_b16 a[54:55], v197 offset:17408\n"
      "ds_read_b64_tr_b16 a[56:57], v198 offset:17408\n"
      "ds_read_b64_tr_b16 a[58:59], v199 offset:17408\n"
      "ds_read_b128 a[72:75], v206 offset:2048\n"
      "ds_read_b64_tr_b16 a[84:85], v200 offset:17920\n"
      "ds_read_b64_tr_b16 a[86:87], v201 offset:17920\n"
      "ds_read_b64_tr_b16 a[88:89], v202 offset:17920\n"
      "ds_read_b64_tr_b16 a[90:91], v203 offset:17920\n"
      "s_waitcnt lgkmcnt(15)\n"
      "v_mfma_f32_16x16x32_bf16 a[0:3], a[224:227], v[80:83], a[0:3]\n"
      "ds_read_b128 a[12:15], v204 offset:3072\n"
      "ds_read_b128 a[44:47], v205 offset:3072\n"
      "v_mfma_f32_16x16x32_bf16 a[4:7], a[228:231], v[80:83], a[4:7]\n"
      "ds_read_b128 a[76:79], v206 offset:3072\n"
      "ds_read_b64 v[112:113], v204 offset:6144\n"
      "s_waitcnt lgkmcnt(15)\n"
      "v_mfma_f32_16x16x32_bf16 v[160:163], a[20:23], a[8:11], v[160:163]\n"
      "ds_read_b64 v[114:115], v205 offset:6144\n"
      "ds_read_b64 v[116:117], v206 offset:6144\n"
      "s_waitcnt lgkmcnt(15)\n"
      "v_mfma_f32_16x16x32_bf16 v[164:167], a[24:27], a[8:11], v[164:167]\n"
      "ds_read_b128 a[16:19], v204 offset:4096\n"
      "ds_read_b128 a[48:51], v205 offset:4096\n"
      "s_waitcnt lgkmcnt(8)\n"
      "v_mfma_f32_16x16x32_bf16 v[160:163], a[52:55], a[40:43], v[160:163]\n"
      "ds_read_b128 a[80:83], v206 offset:4096\n"
      "v_accvgpr_read_b32 v216, a0\n"
      "v_mfma_f32_16x16x32_bf16 v[164:167], a[56:59], a[40:43], v[164:167]\n"
      "v_accvgpr_read_b32 v217, a1\n"
      "v_accvgpr_read_b32 v218, a2\n"
      "v_mfma_f32_16x16x32_bf16 v[160:163], a[84:87], a[72:75], v[160:163]\n"
      "v_accvgpr_read_b32 v219, a3\n"
      "v_accvgpr_read_b32 v220, a4\n"
      "v_mfma_f32_16x16x32_bf16 v[164:167], a[88:91], a[72:75], v[164:167]\n"
      "v_accvgpr_read_b32 v221, a5\n"
      "v_accvgpr_read_b32 v222, a6\n"
      "v_mfma_f32_16x16x32_bf16 a[32:35], a[224:227], v[84:87], a[32:35]\n"
      "v_accvgpr_read_b32 v223, a7\n"
      "s_waitcnt lgkmcnt(3)\n"
      "v_mfma_f32_16x16x32_bf16 a[36:39], a[228:231], v[84:87], a[36:39]\n"
      "v_cvt_pk_bf16_f32 v92, v20, v21\n"
      "v_lshlrev_b32 v210, 2, v11\n"
      "v_mfma_f32_16x16x32_bf16 v[168:171], a[20:23], a[12:15], v[168:171]\n"
      "v_add_f32 v160, v160, v216\n"
      "v_add_f32 v161, v161, v217\n"
      "v_add_f32 v162, v162, v218\n"
      "v_add_f32 v163, v163, v219\n"
      "v_mfma_f32_16x16x32_bf16 v[172:175], a[24:27], a[12:15], v[172:175]\n"
      "v_add_f32 v164, v164, v220\n"
      "v_add_f32 v165, v165, v221\n"
      "v_add_f32 v166, v166, v222\n"
      "v_mfma_f32_16x16x32_bf16 v[168:171], a[52:55], a[44:47], v[168:171]\n"
      "v_add_f32 v167, v167, v223\n"
      "v_cvt_pk_bf16_f32 v32, v160, v161\n"
      "v_cvt_pk_bf16_f32 v33, v162, v163\n"
      "v_mfma_f32_16x16x32_bf16 v[172:175], a[56:59], a[44:47], v[172:175]\n"
      "v_cvt_pk_bf16_f32 v34, v164, v165\n"
      "v_cvt_pk_bf16_f32 v35, v166, v167\n"
      "v_mfma_f32_16x16x32_bf16 v[168:171], a[84:87], a[76:79], v[168:171]\n"
      "buffer_store_dwordx4 v[32:35], v14, s[16:19], s46 offen offset:0 sc1\n"
      "v_accvgpr_read_b32 v184, a32\n"
      "v_accvgpr_read_b32 v185, a33\n"
      "v_accvgpr_read_b32 v186, a34\n"
      "v_mfma_f32_16x16x32_bf16 v[172:175], a[88:91], a[76:79], v[172:175]\n"
      "v_accvgpr_read_b32 v187, a35\n"
      "v_accvgpr_read_b32 v188, a36\n"
      "v_mfma_f32_16x16x32_bf16 a[64:67], a[224:227], v[88:91], a[64:67]\n"
      "v_accvgpr_read_b32 v189, a37\n"
      "v_accvgpr_read_b32 v190, a38\n"
      "v_mfma_f32_16x16x32_bf16 a[68:71], a[228:231], v[88:91], a[68:71]\n"
      "v_accvgpr_read_b32 v191, a39\n"
      "v_add_f32 v112, v112, v16\n"
      "s_waitcnt lgkmcnt(0)\n"
      "v_mfma_f32_16x16x32_bf16 v[176:179], a[20:23], a[16:19], v[176:179]\n"
      "v_add_f32 v168, v168, v184\n"
      "v_add_f32 v169, v169, v185\n"
      "v_add_f32 v170, v170, v186\n"
      "v_add_f32 v171, v171, v187\n"
      "v_mfma_f32_16x16x32_bf16 v[180:183], a[24:27], a[16:19], v[180:183]\n"
      "v_add_f32 v172, v172, v188\n"
      "v_add_f32 v173, v173, v189\n"
      "v_add_f32 v174, v174, v190\n"
      "v_mfma_f32_16x16x32_bf16 v[176:179], a[52:55], a[48:51], v[176:179]\n"
      "v_add_f32 v175, v175, v191\n"
      "v_cvt_pk_bf16_f32 v36, v168, v169\n"
      "v_cvt_pk_bf16_f32 v37, v170, v171\n"
      "v_add_f32 v114, v114, v116\n"
      "v_mfma_f32_16x16x32_bf16 v[180:183], a[56:59], a[48:51], v[180:183]\n"
      "v_cvt_pk_bf16_f32 v38, v172, v173\n"
      "v_cvt_pk_bf16_f32 v39, v174, v175\n"
      "s_lshl_b32 s52, s43, 31\n"
      "v_mfma_f32_16x16x32_bf16 v[176:179], a[84:87], a[80:83], v[176:179]\n"
      "v_cvt_pk_bf16_f32 v93, v22, v23\n"
      "v_accvgpr_read_b32 v104, a64\n"
      "v_accvgpr_read_b32 v105, a65\n"
      "v_accvgpr_read_b32 v106, a66\n"
      "v_add_f32 v112, v112, v114\n"
      "v_mfma_f32_16x16x32_bf16 v[180:183], a[88:91], a[80:83], v[180:183]\n"
      "buffer_store_dwordx4 v[36:39], v14, s[16:19], s46 offen offset:1024 sc1\n"
      "v_exp_f32 v156, v62\n"
      "v_accvgpr_read_b32 v107, a67\n"
      "v_accvgpr_read_b32 v108, a68\n"
      "v_or_b32 v112, s52, v112\n"
      "v_mfma_f32_16x16x32_bf16 v[64:67], a[192:195], v[224:227], v[120:123]\n"
      "v_accvgpr_read_b32 v109, a69\n"
      "v_accvgpr_read_b32 v110, a70\n"
      "v_mfma_f32_16x16x32_bf16 v[72:75], a[192:195], v[240:243], v[124:127]\n"
      "buffer_store_dword v112, v210, s[16:19], s46 offen offset:3072 sc1\n"
      "v_accvgpr_read_b32 v111, a71\n"
      "v_mfma_f32_16x16x32_bf16 v[68:71], a[196:199], v[224:227], v[120:123]\n"
      "ds_read_b64_tr_b16 a[232:233], v7 offset:16896\n"
      "v_exp_f32 v157, v63\n"
      "v_add_f32 v176, v176, v104\n"
      "v_add_f32 v177, v177, v105\n"
      "v_add_f32 v178, v178, v106\n"
      "v_add_f32 v179, v179, v107\n"
      "v_mfma_f32_16x16x32_bf16 v[76:79], a[196:199], v[240:243], v[124:127]\n"
      "ds_read_b64_tr_b16 a[234:235], v8 offset:16896\n"
      "v_cvt_pk_bf16_f32 v94, v24, v25\n"
      "v_add_f32 v180, v180, v108\n"
      "v_add_f32 v181, v181, v109\n"
      "v_add_f32 v182, v182, v110\n"
      "v_mfma_f32_16x16x32_bf16 v[64:67], a[200:203], v[228:231], v[64:67]\n"
      "ds_read_b64_tr_b16 a[236:237], v9 offset:16896\n"
      "ds_read_b64_tr_b16 a[238:239], v10 offset:16896\n"
      "v_add_f32 v183, v183, v111\n"
      "v_cvt_pk_bf16_f32 v40, v176, v177\n"
      "v_cvt_pk_bf16_f32 v41, v178, v179\n"
      "v_mfma_f32_16x16x32_bf16 v[72:75], a[200:203], v[244:247], v[72:75]\n"
      "ds_read_b64_tr_b16 a[240:241], v7 offset:17408\n"
      "v_cvt_pk_bf16_f32 v42, v180, v181\n"
      "v_cvt_pk_bf16_f32 v43, v182, v183\n"
      "v_mfma_f32_16x16x32_bf16 v[68:71], a[204:207], v[228:231], v[68:71]\n"
      "ds_read_b64_tr_b16 a[242:243], v8 offset:17408\n"
      "v_cvt_pk_bf16_f32 v95, v156, v157\n"
      "v_mfma_f32_16x16x32_bf16 v[76:79], a[204:207], v[244:247], v[76:79]\n"
      "buffer_store_dwordx4 v[40:43], v14, s[16:19], s46 offen offset:2048 sc1\n"
      "ds_read_b64_tr_b16 a[244:245], v9 offset:17408\n"
      "v_mfma_f32_16x16x32_bf16 v[140:143], v[152:155], v[92:95], v[140:143]\n"
      "ds_read_b64_tr_b16 a[246:247], v10 offset:17408\n"
      "v_or3_b32 v18, v18, v113, v115\n"
      "v_mfma_f32_16x16x32_bf16 v[64:67], a[208:211], v[232:235], v[64:67]\n"
      "ds_read_b64_tr_b16 a[248:249], v7 offset:17920\n"
      "ds_read_b64_tr_b16 a[250:251], v8 offset:17920\n"
      "v_mfma_f32_16x16x32_bf16 v[72:75], a[208:211], v[248:251], v[72:75]\n"
      "ds_read_b64_tr_b16 a[252:253], v9 offset:17920\n"
      "ds_read_b64_tr_b16 a[254:255], v10 offset:17920\n"
      "v_mfma_f32_16x16x32_bf16 v[68:71], a[212:215], v[232:235], v[68:71]\n"
      "v_or_b32 v18, v18, v117\n"
      "v_mfma_f32_16x16x32_bf16 v[76:79], a[212:215], v[248:251], v[76:79]\n"
      "v_cmp_lt_u32 vcc, s43, v18\n"
      "s_cmp_eq_u32 s43, 1\n"
      "s_cbranch_vccnz FALLBACK_%=\n"
      "s_cbranch_scc0 NOREF_%=\n"
      "buffer_store_dword v12, v210, s[16:19], s46 offen offset:3328 sc1\n"
      "NOREF_%=:\n"
      "v_mfma_f32_16x16x32_bf16 v[64:67], a[216:219], v[236:239], v[64:67]\n"
      "v_bfe_u32 v213, v11, 2, 2\n"
      "v_and_b32 v214, 3, v11\n"
      "v_mfma_f32_16x16x32_bf16 v[72:75], a[216:219], v[252:255], v[72:75]\n"
      "v_lshl_add_u32 v213, v213, 3, v214\n"
      "v_lshlrev_b32 v213, 8, v213\n"
      "v_mfma_f32_16x16x32_bf16 v[68:71], a[220:223], v[236:239], v[68:71]\n"
      "v_bfe_u32 v214, v11, 4, 1\n"
      "v_lshl_add_u32 v213, v214, 5, v213\n"
      "v_mfma_f32_16x16x32_bf16 v[76:79], a[220:223], v[252:255], v[76:79]\n"
      "v_lshrrev_b32 v214, 5, v11\n"
      "v_lshl_add_u32 v213, v214, 4, v213\n"
      "s_waitcnt lgkmcnt(0)\n"
      "v_mfma_f32_16x16x32_bf16 a[104:107], a[232:235], v[92:95], a[104:107]\n"
      "s_lshl_b32 s52, s36, 6\n"
      "v_add_u32 v212, s52, v213\n"
      "v_mfma_f32_16x16x32_bf16 a[108:111], a[236:239], v[92:95], a[108:111]\n"
      "v_exp_f32 v20, v64\n"
      "v_exp_f32 v21, v65\n"
      "v_exp_f32 v22, v66\n"
      "v_mfma_f32_16x16x32_bf16 a[112:115], a[240:243], v[92:95], a[112:115]\n"
      "v_exp_f32 v23, v67\n"
      "v_mfma_f32_16x16x32_bf16 a[116:119], a[244:247], v[92:95], a[116:119]\n"
      "v_exp_f32 v24, v68\n"
      "v_mfma_f32_16x16x32_bf16 a[120:123], a[248:251], v[92:95], a[120:123]\n"
      "v_exp_f32 v25, v69\n"
      "v_mfma_f32_16x16x32_bf16 a[124:127], a[252:255], v[92:95], a[124:127]\n"
      "v_exp_f32 v156, v70\n"
      "v_mfma_f32_16x16x32_bf16 a[96:99], a[224:227], v[92:95], a[96:99]\n"
      "v_cvt_pk_bf16_f32 v96, v20, v21\n"
      "v_exp_f32 v157, v71\n"
      "v_mfma_f32_16x16x32_bf16 a[100:103], a[228:231], v[92:95], a[100:103]\n"
      "ds_write_b128 v30, a[104:107] offset:0\n"
      "v_exp_f32 v20, v72\n"
      "ds_write_b128 v30, a[108:111] offset:1024\n"
      "v_cvt_pk_bf16_f32 v97, v22, v23\n"
      "v_cvt_pk_bf16_f32 v98, v24, v25\n"
      "v_cvt_pk_bf16_f32 v99, v156, v157\n"
      "v_exp_f32 v21, v73\n"
      "s_nop 0\n"
      "v_mfma_f32_16x16x32_bf16 a[136:139], a[232:235], v[96:99], a[136:139]\n"
      "ds_write_b128 v30, a[112:115] offset:6144\n"
      "v_exp_f32 v22, v74\n"
      "v_exp_f32 v23, v75\n"
      "v_mfma_f32_16x16x32_bf16 v[144:147], v[152:155], v[96:99], v[144:147]\n"
      "v_exp_f32 v24, v76\n"
      "v_mfma_f32_16x16x32_bf16 a[140:143], a[236:239], v[96:99], a[140:143]\n"
      "ds_write_b128 v30, a[116:119] offset:7168\n"
      "v_mfma_f32_16x16x32_bf16 a[144:147], a[240:243], v[96:99], a[144:147]\n"
      "ds_write_b128 v30, a[120:123] offset:12288\n"
      "v_exp_f32 v25, v77\n"
      "v_mfma_f32_16x16x32_bf16 a[148:151], a[244:247], v[96:99], a[148:151]\n"
      "ds_write_b128 v30, a[124:127] offset:13312\n"
      "v_exp_f32 v156, v78\n"
      "v_mfma_f32_16x16x32_bf16 a[152:155], a[248:251], v[96:99], a[152:155]\n"
      "s_waitcnt vmcnt(0)\n"
      "s_mov_b64 exec, 1\n"
      "buffer_store_dword v19, off, s[16:19], s48 offset:4 sc1\n"
      "s_mov_b64 exec, -1\n"
      "v_exp_f32 v157, v79\n"
      "v_mfma_f32_16x16x32_bf16 a[156:159], a[252:255], v[96:99], a[156:159]\n"
      "v_cvt_pk_bf16_f32 v100, v20, v21\n"
      "v_cvt_pk_bf16_f32 v101, v22, v23\n"
      "v_mfma_f32_16x16x32_bf16 a[128:131], a[224:227], v[96:99], a[128:131]\n"
      "ds_write_b128 v30, a[136:139] offset:2048\n"
      "v_mfma_f32_16x16x32_bf16 a[132:135], a[228:231], v[96:99], a[132:135]\n"
      "v_cvt_pk_bf16_f32 v102, v24, v25\n"
      "v_cvt_pk_bf16_f32 v103, v156, v157\n"
      "s_nop 1\n"
      "v_mfma_f32_16x16x32_bf16 a[168:171], a[232:235], v[100:103], a[168:171]\n"
      "ds_write_b128 v30, a[140:143] offset:3072\n"
      "v_mfma_f32_16x16x32_bf16 v[148:151], v[152:155], v[100:103], v[148:151]\n"
      "v_accvgpr_read_b32 v160, a96\n"
      "v_accvgpr_read_b32 v161, a97\n"
      "v_mfma_f32_16x16x32_bf16 a[172:175], a[236:239], v[100:103], a[172:175]\n"
      "ds_write_b128 v30, a[144:147] offset:8192\n"
      "v_mfma_f32_16x16x32_bf16 a[176:179], a[240:243], v[100:103], a[176:179]\n"
      "ds_write_b128 v30, a[148:151] offset:9216\n"
      "v_mfma_f32_16x16x32_bf16 a[180:183], a[244:247], v[100:103], a[180:183]\n"
      "ds_write_b128 v30, a[152:155] offset:14336\n"
      "v_mfma_f32_16x16x32_bf16 a[184:187], a[248:251], v[100:103], a[184:187]\n"
      "ds_write_b128 v30, a[156:159] offset:15360\n"
      "v_cndmask_b32 v17, v140, v144, s[56:57]\n"
      "v_cndmask_b32 v17, v17, v148, s[58:59]\n"
      "v_mfma_f32_16x16x32_bf16 a[188:191], a[252:255], v[100:103], a[188:191]\n"
      "v_med3_f32 v18, v17, v15, s42\n"
      "v_accvgpr_read_b32 v162, a98\n"
      "v_mfma_f32_16x16x32_bf16 a[160:163], a[224:227], v[100:103], a[160:163]\n"
      "v_cmp_neq_f32 vcc, v18, v17\n"
      "s_nop 0\n"
      "s_cmp_lg_u64 vcc, 0\n"
      "s_cselect_b32 s45, 1, 0\n"
      "v_mov_b32 v18, s45\n"
      "v_mfma_f32_16x16x32_bf16 a[164:167], a[228:231], v[100:103], a[164:167]\n"
      "ds_write_b128 v30, a[168:171] offset:4096\n"
      "ds_write2_b32 v158, v17, v18 offset0:2 offset1:3\n"
      "ds_write_b128 v30, a[172:175] offset:5120\n"
      "ds_write_b128 v30, a[176:179] offset:10240\n"
      "ds_write_b128 v30, a[180:183] offset:11264\n"
      "ds_write_b128 v31, a[184:187] offset:0\n"
      "ds_write_b128 v31, a[188:191] offset:1024\n"
      "s_mov_b32 s53, 0x2400\n"
      "s_cmp_eq_u32 s37, 0\n"
      "s_cselect_b32 s60, 0x0, s53\n"
      "s_mov_b32 s53, 0x4000\n"
      "s_cmp_eq_u32 s37, 0\n"
      "s_cselect_b32 s61, 0x400, s53\n"
      "s_mov_b32 s53, 0x4400\n"
      "s_cmp_eq_u32 s37, 0\n"
      "s_cselect_b32 s62, 0x2000, s53\n"
      "s_add_u32 s28, s10, s50\n"
      "s_addc_u32 s29, s11, 0\n"
      "s_and_b32 s29, s29, 0xffff\n"
      "v_accvgpr_read_b32 v163, a99\n"
      "v_accvgpr_read_b32 v164, a100\n"
      "v_accvgpr_read_b32 v165, a101\n"
      "v_accvgpr_read_b32 v166, a102\n"
      "v_accvgpr_read_b32 v167, a103\n"
      "v_accvgpr_read_b32 v168, a128\n"
      "v_accvgpr_read_b32 v169, a129\n"
      "v_accvgpr_read_b32 v170, a130\n"
      "v_accvgpr_read_b32 v171, a131\n"
      "v_accvgpr_read_b32 v172, a132\n"
      "v_accvgpr_read_b32 v173, a133\n"
      "v_accvgpr_read_b32 v174, a134\n"
      "v_accvgpr_read_b32 v175, a135\n"
      "buffer_load_dwordx2 v[208:209], off, s[16:19], s49 sc1\n"
      "v_accvgpr_read_b32 v176, a160\n"
      "v_accvgpr_read_b32 v177, a161\n"
      "v_accvgpr_read_b32 v178, a162\n"
      "v_accvgpr_read_b32 v179, a163\n"
      "v_accvgpr_read_b32 v180, a164\n"
      "v_accvgpr_read_b32 v181, a165\n"
      "v_accvgpr_read_b32 v182, a166\n"
      "v_accvgpr_read_b32 v183, a167\n"
      "s_mov_b32 s45, 0x40000\n"
      "s_waitcnt lgkmcnt(0)\n"
      "s_barrier\n"
      "ds_read_b64 v[44:45], v204 offset:6152\n"
      "ds_read_b64 v[46:47], v205 offset:6152\n"
      "ds_read_b64 v[48:49], v206 offset:6152\n"
      "ds_read_b128 v[56:59], v26 offset:0\n"
      "ds_read_b128 v[60:63], v26 offset:1024\n"
      "ds_read_b128 v[80:83], v27 offset:6144\n"
      "ds_read_b128 v[84:87], v27 offset:7168\n"
      "ds_read_b128 v[104:107], v28 offset:12288\n"
      "ds_read_b128 v[108:111], v28 offset:13312\n"
      "ds_read_b128 v[64:67], v26 offset:2048\n"
      "ds_read_b128 v[68:71], v26 offset:3072\n"
      "ds_read_b128 v[88:91], v27 offset:8192\n"
      "ds_read_b128 v[92:95], v27 offset:9216\n"
      "ds_read_b128 v[112:115], v28 offset:14336\n"
      "ds_read_b128 v[116:119], v28 offset:15360\n"
      "s_waitcnt vmcnt(0)\n"
      "v_cmp_ne_u32 vcc, s44, v209\n"
      "s_mov_b32 s55, 0\n"
      "s_cbranch_vccnz NOTYET_%=\n"
      "buffer_load_dword v25, v210, s[16:19], s47 offen offset:3072 sc1\n"
      "buffer_load_dwordx4 v[140:143], v14, s[16:19], s47 offen offset:0 sc1\n"
      "buffer_load_dwordx4 v[144:147], v14, s[16:19], s47 offen offset:1024 sc1\n"
      "buffer_load_dwordx4 v[148:151], v14, s[16:19], s47 offen offset:2048 sc1\n"
      "s_mov_b32 s55, 1\n"
      "s_branch EARLY_%=\n"
      "NOTYET_%=:\n"
      "buffer_load_dwordx2 v[208:209], off, s[16:19], s49 sc1\n"
      "EARLY_%=:\n"
      "s_waitcnt lgkmcnt(12)\n"
      "v_or3_b32 v18, v18, v45, v47\n"
      "v_add_f32 v17, v17, v44\n"
      "v_or_b32 v18, v18, v49\n"
      "v_add_f32 v46, v46, v48\n"
      "v_cmp_lt_u32 vcc, s43, v18\n"
      "v_add_f32 v17, v17, v46\n"
      "s_cbranch_vccnz FALLBACK2_%=\n"
      "s_waitcnt lgkmcnt(8)\n"
      "v_pk_add_f32 v[56:57], v[56:57], v[80:81]\n"
      "v_pk_add_f32 v[58:59], v[58:59], v[82:83]\n"
      "v_pk_add_f32 v[60:61], v[60:61], v[84:85]\n"
      "v_pk_add_f32 v[62:63], v[62:63], v[86:87]\n"
      "s_waitcnt lgkmcnt(6)\n"
      "v_pk_add_f32 v[160:161], v[160:161], v[104:105]\n"
      "v_pk_add_f32 v[162:163], v[162:163], v[106:107]\n"
      "v_pk_add_f32 v[164:165], v[164:165], v[108:109]\n"
      "v_pk_add_f32 v[166:167], v[166:167], v[110:111]\n"
      "v_pk_add_f32 v[160:161], v[160:161], v[56:57]\n"
      "v_pk_add_f32 v[162:163], v[162:163], v[58:59]\n"
      "v_pk_add_f32 v[164:165], v[164:165], v[60:61]\n"
      "v_pk_add_f32 v[166:167], v[166:167], v[62:63]\n"
      "ds_read_b128 v[72:75], v26 offset:4096\n"
      "ds_read_b128 v[76:79], v26 offset:5120\n"
      "ds_read_b128 v[96:99], v27 offset:10240\n"
      "ds_read_b128 v[100:103], v27 offset:11264\n"
      "ds_read_b128 v[120:123], v29 offset:0\n"
      "ds_read_b128 v[124:127], v29 offset:1024\n"
      "s_waitcnt lgkmcnt(8)\n"
      "v_pk_add_f32 v[64:65], v[64:65], v[88:89]\n"
      "v_pk_add_f32 v[66:67], v[66:67], v[90:91]\n"
      "v_pk_add_f32 v[68:69], v[68:69], v[92:93]\n"
      "v_pk_add_f32 v[70:71], v[70:71], v[94:95]\n"
      "s_waitcnt lgkmcnt(6)\n"
      "v_pk_add_f32 v[168:169], v[168:169], v[112:113]\n"
      "v_pk_add_f32 v[170:171], v[170:171], v[114:115]\n"
      "v_pk_add_f32 v[172:173], v[172:173], v[116:117]\n"
      "v_pk_add_f32 v[174:175], v[174:175], v[118:119]\n"
      "v_pk_add_f32 v[168:169], v[168:169], v[64:65]\n"
      "v_pk_add_f32 v[170:171], v[170:171], v[66:67]\n"
      "v_pk_add_f32 v[172:173], v[172:173], v[68:69]\n"
      "v_pk_add_f32 v[174:175], v[174:175], v[70:71]\n"
      "s_waitcnt lgkmcnt(2)\n"
      "v_pk_add_f32 v[72:73], v[72:73], v[96:97]\n"
      "v_pk_add_f32 v[74:75], v[74:75], v[98:99]\n"
      "v_pk_add_f32 v[76:77], v[76:77], v[100:101]\n"
      "v_pk_add_f32 v[78:79], v[78:79], v[102:103]\n"
      "s_waitcnt lgkmcnt(0)\n"
      "v_pk_add_f32 v[176:177], v[176:177], v[120:121]\n"
      "v_pk_add_f32 v[178:179], v[178:179], v[122:123]\n"
      "v_pk_add_f32 v[180:181], v[180:181], v[124:125]\n"
      "v_pk_add_f32 v[182:183], v[182:183], v[126:127]\n"
      "v_pk_add_f32 v[176:177], v[176:177], v[72:73]\n"
      "v_pk_add_f32 v[178:179], v[178:179], v[74:75]\n"
      "v_pk_add_f32 v[180:181], v[180:181], v[76:77]\n"
      "v_pk_add_f32 v[182:183], v[182:183], v[78:79]\n"
      "s_cmp_eq_u32 s55, 1\n"
      "s_cbranch_scc1 HAVE_%=\n"
      "s_branch POLLW_%=\n"
      "POLL_%=:\n"
      "buffer_load_dwordx2 v[208:209], off, s[16:19], s49 sc1\n"
      "POLLW_%=:\n"
      "s_waitcnt vmcnt(0)\n"
      "v_cmp_ne_u32 vcc, s44, v209\n"
      "s_sub_u32 s45, s45, 1\n"
      "s_cbranch_vccz GOT_%=\n"
      "s_cbranch_scc0 POLL_%=\n"
      "GOT_%=:\n"
      "buffer_load_dword v25, v210, s[16:19], s47 offen offset:3072 sc1\n"
      "buffer_load_dwordx4 v[140:143], v14, s[16:19], s47 offen offset:0 sc1\n"
      "buffer_load_dwordx4 v[144:147], v14, s[16:19], s47 offen offset:1024 sc1\n"
      "buffer_load_dwordx4 v[148:151], v14, s[16:19], s47 offen offset:2048 sc1\n"
      "HAVE_%=:\n"
      "v_cmp_ne_u32 vcc, v208, v19\n"
      "s_cbranch_vccz NOFIX_%=\n"
      "s_mov_b64 exec, 1\n"
      "buffer_store_dword v208, off, s[16:19], s48 offset:4 sc1\n"
      "s_mov_b64 exec, -1\n"
      "s_waitcnt vmcnt(0)\n"
      "NOFIX_%=:\n"
      "s_waitcnt vmcnt(3)\n"
      "v_lshrrev_b32 v2, 31, v25\n"
      "v_or_b32 v2, s43, v2\n"
      "v_cmp_ne_u32 vcc, 0, v2\n"
      "v_add_f32 v56, v17, v25\n"
      "s_cbranch_vccnz GENERAL_%=\n"
      "v_rcp_f32 v56, v56\n"
      "s_nop 0\n"
      "v_mov_b32 v57, v56\n"
      "s_nop 1\n"
      "v_permlane16_swap_b32 v56, v57\n"
      "v_mov_b32 v58, v56\n"
      "v_mov_b32 v3, v57\n"
      "s_nop 0\n"
      "v_permlane32_swap_b32 v56, v58\n"
      "v_permlane32_swap_b32 v57, v3\n"
      "s_waitcnt vmcnt(2)\n"
      "v_lshlrev_b32 v32, 16, v140\n"
      "v_and_b32 v33, 0xffff0000, v140\n"
      "v_lshlrev_b32 v34, 16, v141\n"
      "v_and_b32 v35, 0xffff0000, v141\n"
      "v_lshlrev_b32 v36, 16, v142\n"
      "v_and_b32 v37, 0xffff0000, v142\n"
      "v_lshlrev_b32 v38, 16, v143\n"
      "v_and_b32 v39, 0xffff0000, v143\n"
      "v_pk_add_f32 v[160:161], v[160:161], v[32:33]\n"
      "v_pk_add_f32 v[162:163], v[162:163], v[34:35]\n"
      "v_pk_add_f32 v[164:165], v[164:165], v[36:37]\n"
      "v_pk_add_f32 v[166:167], v[166:167], v[38:39]\n"
      "v_pk_mul_f32 v[160:161], v[160:161], v[56:57] op_sel:[0,0] op_sel_hi:[1,0]\n"
      "v_pk_mul_f32 v[162:163], v[162:163], v[56:57] op_sel:[0,0] op_sel_hi:[1,0]\n"
      "v_pk_mul_f32 v[164:165], v[164:165], v[56:57] op_sel:[0,0] op_sel_hi:[1,0]\n"
      "v_pk_mul_f32 v[166:167], v[166:167], v[56:57] op_sel:[0,0] op_sel_hi:[1,0]\n"
      "v_cvt_pk_bf16_f32 v40, v160, v161\n"
      "v_cvt_pk_bf16_f32 v41, v162, v163\n"
      "v_cvt_pk_bf16_f32 v42, v164, v165\n"
      "v_cvt_pk_bf16_f32 v43, v166, v167\n"
      "s_nop 0\n"
      "v_permlane16_swap_b32 v40, v42\n"
      "v_permlane16_swap_b32 v41, v43\n"
      "buffer_store_dwordx4 v[40:43], v212, s[28:31], s60 offen \n"
      "s_waitcnt vmcnt(2)\n"
      "v_lshlrev_b32 v32, 16, v144\n"
      "v_and_b32 v33, 0xffff0000, v144\n"
      "v_lshlrev_b32 v34, 16, v145\n"
      "v_and_b32 v35, 0xffff0000, v145\n"
      "v_lshlrev_b32 v36, 16, v146\n"
      "v_and_b32 v37, 0xffff0000, v146\n"
      "v_lshlrev_b32 v38, 16, v147\n"
      "v_and_b32 v39, 0xffff0000, v147\n"
      "v_pk_add_f32 v[168:169], v[168:169], v[32:33]\n"
      "v_pk_add_f32 v[170:171], v[170:171], v[34:35]\n"
      "v_pk_add_f32 v[172:173], v[172:173], v[36:37]\n"
      "v_pk_add_f32 v[174:175], v[174:175], v[38:39]\n"
      "v_pk_mul_f32 v[168:169], v[168:169], v[56:57] op_sel:[0,1] op_sel_hi:[1,1]\n"
      "v_pk_mul_f32 v[170:171], v[170:171], v[56:57] op_sel:[0,1] op_sel_hi:[1,1]\n"
      "v_pk_mul_f32 v[172:173], v[172:173], v[56:57] op_sel:[0,1] op_sel_hi:[1,1]\n"
      "v_pk_mul_f32 v[174:175], v[174:175], v[56:57] op_sel:[0,1] op_sel_hi:[1,1]\n"
      "v_cvt_pk_bf16_f32 v44, v168, v169\n"
      "v_cvt_pk_bf16_f32 v45, v170, v171\n"
      "v_cvt_pk_bf16_f32 v46, v172, v173\n"
      "v_cvt_pk_bf16_f32 v47, v174, v175\n"
      "s_nop 0\n"
      "v_permlane16_swap_b32 v44, v46\n"
      "v_permlane16_swap_b32 v45, v47\n"
      "buffer_store_dwordx4 v[44:47], v212, s[28:31], s61 offen \n"
      "s_waitcnt vmcnt(2)\n"
      "v_lshlrev_b32 v32, 16, v148\n"
      "v_and_b32 v33, 0xffff0000, v148\n"
      "v_lshlrev_b32 v34, 16, v149\n"
      "v_and_b32 v35, 0xffff0000, v149\n"
      "v_lshlrev_b32 v36, 16, v150\n"
      "v_and_b32 v37, 0xffff0000, v150\n"
      "v_lshlrev_b32 v38, 16, v151\n"
      "v_and_b32 v39, 0xffff0000, v151\n"
      "v_pk_add_f32 v[176:177], v[176:177], v[32:33]\n"
      "v_pk_add_f32 v[178:179], v[178:179], v[34:35]\n"
      "v_pk_add_f32 v[180:181], v[180:181], v[36:37]\n"
      "v_pk_add_f32 v[182:183], v[182:183], v[38:39]\n"
      "v_pk_mul_f32 v[176:177], v[176:177], v[58:59] op_sel:[0,0] op_sel_hi:[1,0]\n"
      "v_pk_mul_f32 v[178:179], v[178:179], v[58:59] op_sel:[0,0] op_sel_hi:[1,0]\n"
      "v_pk_mul_f32 v[180:181], v[180:181], v[58:59] op_sel:[0,0] op_sel_hi:[1,0]\n"
      "v_pk_mul_f32 v[182:183], v[182:183], v[58:59] op_sel:[0,0] op_sel_hi:[1,0]\n"
      "v_cvt_pk_bf16_f32 v48, v176, v177\n"
      "v_cvt_pk_bf16_f32 v49, v178, v179\n"
      "v_cvt_pk_bf16_f32 v50, v180, v181\n"
      "v_cvt_pk_bf16_f32 v51, v182, v183\n"
      "s_nop 0\n"
      "v_permlane16_swap_b32 v48, v50\n"
      "v_permlane16_swap_b32 v49, v51\n"
      "buffer_store_dwordx4 v[48:51], v212, s[28:31], s62 offen \n"
      "DONE_%=:\n"
      "s_endpgm\n"
      "CODEEND_%=:\n"
      "GENERAL_%=:\n"
      "buffer_load_dword v4, v210, s[16:19], s47 offen offset:3328 sc1\n"
      "s_waitcnt vmcnt(0)\n"
      "v_cmp_gt_i32 vcc, 0, v25\n"
      "s_nop 1\n"
      "v_cndmask_b32 v4, 0, v4, vcc\n"
      "v_and_b32 v25, 0x7fffffff, v25\n"
      "v_max_f32 v5, v13, v4\n"
      "v_sub_f32 v56, v13, v5\n"
      "v_sub_f32 v60, v4, v5\n"
      "v_exp_f32 v56, v56\n"
      "v_exp_f32 v60, v60\n"
      "v_mul_f32 v5, v56, v17\n"
      "v_fmac_f32 v5, v60, v25\n"
      "v_rcp_f32 v5, v5\n"
      "s_nop 0\n"
      "v_mul_f32 v56, v56, v5\n"
      "v_mul_f32 v60, v60, v5\n"
      "v_mov_b32 v57, v56\n"
      "s_nop 1\n"
      "v_permlane16_swap_b32 v56, v57\n"
      "v_mov_b32 v58, v56\n"
      "v_mov_b32 v3, v57\n"
      "s_nop 0\n"
      "v_permlane32_swap_b32 v56, v58\n"
      "v_permlane32_swap_b32 v57, v3\n"
      "v_mov_b32 v61, v60\n"
      "s_nop 1\n"
      "v_permlane16_swap_b32 v60, v61\n"
      "v_mov_b32 v62, v60\n"
      "v_mov_b32 v3, v61\n"
      "s_nop 0\n"
      "v_permlane32_swap_b32 v60, v62\n"
      "v_permlane32_swap_b32 v61, v3\n"
      "v_lshlrev_b32 v32, 16, v140\n"
      "v_and_b32 v33, 0xffff0000, v140\n"
      "v_lshlrev_b32 v34, 16, v141\n"
      "v_and_b32 v35, 0xffff0000, v141\n"
      "v_lshlrev_b32 v36, 16, v142\n"
      "v_and_b32 v37, 0xffff0000, v142\n"
      "v_lshlrev_b32 v38, 16, v143\n"
      "v_and_b32 v39, 0xffff0000, v143\n"
      "v_pk_mul_f32 v[160:161], v[160:161], v[56:57] op_sel:[0,0] op_sel_hi:[1,0]\n"
      "v_pk_mul_f32 v[162:163], v[162:163], v[56:57] op_sel:[0,0] op_sel_hi:[1,0]\n"
      "v_pk_mul_f32 v[164:165], v[164:165], v[56:57] op_sel:[0,0] op_sel_hi:[1,0]\n"
      "v_pk_mul_f32 v[166:167], v[166:167], v[56:57] op_sel:[0,0] op_sel_hi:[1,0]\n"
      "v_pk_fma_f32 v[160:161], v[32:33], v[60:61], v[160:161] op_sel:[0,0,0] op_sel_hi:[1,0,1]\n"
      "v_pk_fma_f32 v[162:163], v[34:35], v[60:61], v[162:163] op_sel:[0,0,0] op_sel_hi:[1,0,1]\n"
      "v_pk_fma_f32 v[164:165], v[36:37], v[60:61], v[164:165] op_sel:[0,0,0] op_sel_hi:[1,0,1]\n"
      "v_pk_fma_f32 v[166:167], v[38:39], v[60:61], v[166:167] op_sel:[0,0,0] op_sel_hi:[1,0,1]\n"
      "v_cvt_pk_bf16_f32 v40, v160, v161\n"
      "v_cvt_pk_bf16_f32 v41, v162, v163\n"
      "v_cvt_pk_bf16_f32 v42, v164, v165\n"
      "v_cvt_pk_bf16_f32 v43, v166, v167\n"
      "s_nop 0\n"
      "v_permlane16_swap_b32 v40, v42\n"
      "v_permlane16_swap_b32 v41, v43\n"
      "buffer_store_dwordx4 v[40:43], v212, s[28:31], s60 offen \n"
      "v_lshlrev_b32 v32, 16, v144\n"
      "v_and_b32 v33, 0xffff0000, v144\n"
      "v_lshlrev_b32 v34, 16, v145\n"
      "v_and_b32 v35, 0xffff0000, v145\n"
      "v_lshlrev_b32 v36, 16, v146\n"
      "v_and_b32 v37, 0xffff0000, v146\n"
      "v_lshlrev_b32 v38, 16, v147\n"
      "v_and_b32 v39, 0xffff0000, v147\n"
      "v_pk_mul_f32 v[168:169], v[168:169], v[56:57] op_sel:[0,1] op_sel_hi:[1,1]\n"
      "v_pk_mul_f32 v[170:171], v[170:171], v[56:57] op_sel:[0,1] op_sel_hi:[1,1]\n"
      "v_pk_mul_f32 v[172:173], v[172:173], v[56:57] op_sel:[0,1] op_sel_hi:[1,1]\n"
      "v_pk_mul_f32 v[174:175], v[174:175], v[56:57] op_sel:[0,1] op_sel_hi:[1,1]\n"
      "v_pk_fma_f32 v[168:169], v[32:33], v[60:61], v[168:169] op_sel:[0,1,0] op_sel_hi:[1,1,1]\n"
      "v_pk_fma_f32 v[170:171], v[34:35], v[60:61], v[170:171] op_sel:[0,1,0] op_sel_hi:[1,1,1]\n"
      "v_pk_fma_f32 v[172:173], v[36:37], v[60:61], v[172:173] op_sel:[0,1,0] op_sel_hi:[1,1,1]\n"
      "v_pk_fma_f32 v[174:175], v[38:39], v[60:61], v[174:175] op_sel:[0,1,0] op_sel_hi:[1,1,1]\n"
      "v_cvt_pk_bf16_f32 v44, v168, v169\n"
      "v_cvt_pk_bf16_f32 v45, v170, v171\n"
      "v_cvt_pk_bf16_f32 v46, v172, v173\n"
      "v_cvt_pk_bf16_f32 v47, v174, v175\n"
      "s_nop 0\n"
      "v_permlane16_swap_b32 v44, v46\n"
      "v_permlane16_swap_b32 v45, v47\n"
      "buffer_store_dwordx4 v[44:47], v212, s[28:31], s61 offen \n"
      "v_lshlrev_b32 v32, 16, v148\n"
      "v_and_b32 v33, 0xffff0000, v148\n"
      "v_lshlrev_b32 v34, 16, v149\n"
      "v_and_b32 v35, 0xffff0000, v149\n"
      "v_lshlrev_b32 v36, 16, v150\n"
      "v_and_b32 v37, 0xffff0000, v150\n"
      "v_lshlrev_b32 v38, 16, v151\n"
      "v_and_b32 v39, 0xffff0000, v151\n"
      "v_pk_mul_f32 v[176:177], v[176:177], v[58:59] op_sel:[0,0] op_sel_hi:[1,0]\n"
      "v_pk_mul_f32 v[178:179], v[178:179], v[58:59] op_sel:[0,0] op_sel_hi:[1,0]\n"
      "v_pk_mul_f32 v[180:181], v[180:181], v[58:59] op_sel:[0,0] op_sel_hi:[1,0]\n"
      "v_pk_mul_f32 v[182:183], v[182:183], v[58:59] op_sel:[0,0] op_sel_hi:[1,0]\n"
      "v_pk_fma_f32 v[176:177], v[32:33], v[62:63], v[176:177] op_sel:[0,0,0] op_sel_hi:[1,0,1]\n"
      "v_pk_fma_f32 v[178:179], v[34:35], v[62:63], v[178:179] op_sel:[0,0,0] op_sel_hi:[1,0,1]\n"
      "v_pk_fma_f32 v[180:181], v[36:37], v[62:63], v[180:181] op_sel:[0,0,0] op_sel_hi:[1,0,1]\n"
      "v_pk_fma_f32 v[182:183], v[38:39], v[62:63], v[182:183] op_sel:[0,0,0] op_sel_hi:[1,0,1]\n"
      "v_cvt_pk_bf16_f32 v48, v176, v177\n"
      "v_cvt_pk_bf16_f32 v49, v178, v179\n"
      "v_cvt_pk_bf16_f32 v50, v180, v181\n"
      "v_cvt_pk_bf16_f32 v51, v182, v183\n"
      "s_nop 0\n"
      "v_permlane16_swap_b32 v48, v50\n"
      "v_permlane16_swap_b32 v49, v51\n"
      "buffer_store_dwordx4 v[48:51], v212, s[28:31], s62 offen \n"
      "s_branch DONE_%=\n"
      "FALLBACK2_%=:\n"
      "s_add_u32 s46, s46, 4194304\n"
      "s_waitcnt vmcnt(0)\n"
      "FALLBACK_%=:\n"
      "s_mov_b32 s43, 1\n"
      "s_mov_b32 s63, s65\n"
      "s_mov_b32 s53, 7\n"
      "s_add_u32 s52, s40, 0x1000\n"
      "v_mov_b32 v128, 0xff61b1e6\n"
      "v_mov_b32 v129, 0xff61b1e6\n"
      "v_mov_b32 v130, 0xff61b1e6\n"
      "v_mov_b32 v131, 0xff61b1e6\n"
      "v_mov_b32 v132, 0xff61b1e6\n"
      "v_mov_b32 v133, 0xff61b1e6\n"
      "s_waitcnt lgkmcnt(0)\n"
      "s_barrier\n"
      "s_add_u32 s28, s4, s50\n"
      "s_addc_u32 s29, s5, 0\n"
      "s_and_b32 s29, s29, 0xffff\n"
      "s_lshl_b32 s60, s36, 11\n"
      "s_add_u32 s61, s60, 0x2000\n"
      "s_add_u32 s62, s60, 0x4000\n"
      "v_bfe_u32 v20, v11, 2, 3\n"
      "v_and_b32 v21, 3, v11\n"
      "v_lshrrev_b32 v22, 5, v11\n"
      "v_lshrrev_b32 v23, 2, v20\n"
      "v_xor_b32 v21, v21, v23\n"
      "v_lshlrev_b32 v20, 8, v20\n"
      "v_lshl_add_u32 v20, v21, 4, v20\n"
      "s_and_b32 s54, s36, 1\n"
      "s_lshl_b32 s54, s54, 5\n"
      "v_xor_b32 v24, s54, v20\n"
      "v_lshl_add_u32 v24, v22, 6, v24\n"
      "buffer_load_dwordx4 v[32:35], v24, s[28:31], s60 offen\n"
      "buffer_load_dwordx4 v[36:39], v24, s[28:31], s60 offen offset:128\n"
      "buffer_load_dwordx4 v[40:43], v24, s[28:31], s61 offen\n"
      "buffer_load_dwordx4 v[44:47], v24, s[28:31], s61 offen offset:128\n"
      "buffer_load_dwordx4 v[48:51], v24, s[28:31], s62 offen\n"
      "buffer_load_dwordx4 v[52:55], v24, s[28:31], s62 offen offset:128\n"
      "v_subrev_u32 v22, s40, v5\n"
      "v_and_b32 v21, 0x7ff, v22\n"
      "v_lshrrev_b32 v22, 11, v22\n"
      "v_lshl_add_u32 v22, v22, 13, v21\n"
      "v_add_u32 v22, 131072, v22\n"
      "s_xor_b32 s54, s36, 0\n"
      "s_lshl_b32 s54, s54, 9\n"
      "v_add_u32 v64, s54, v22\n"
      "v_xor_b32 v65, 0x110, v64\n"
      "s_xor_b32 s54, s36, 1\n"
      "s_lshl_b32 s54, s54, 9\n"
      "v_add_u32 v66, s54, v22\n"
      "v_xor_b32 v67, 0x110, v66\n"
      "s_xor_b32 s54, s36, 2\n"
      "s_lshl_b32 s54, s54, 9\n"
      "v_add_u32 v68, s54, v22\n"
      "v_xor_b32 v69, 0x110, v68\n"
      "s_xor_b32 s54, s36, 3\n"
      "s_lshl_b32 s54, s54, 9\n"
      "v_add_u32 v70, s54, v22\n"
      "v_xor_b32 v71, 0x110, v70\n"
      "s_waitcnt vmcnt(0)\n"
      "v_lshlrev_b32 v56, 16, v32\n"
      "v_and_b32 v57, 0xffff0000, v32\n"
      "v_pk_mul_f32 v[56:57], v[56:57], s[34:35]\n"
      "v_cvt_pk_bf16_f32 v32, v56, v57\n"
      "v_lshlrev_b32 v58, 16, v33\n"
      "v_and_b32 v59, 0xffff0000, v33\n"
      "v_pk_mul_f32 v[58:59], v[58:59], s[34:35]\n"
      "v_cvt_pk_bf16_f32 v33, v58, v59\n"
      "v_lshlrev_b32 v56, 16, v34\n"
      "v_and_b32 v57, 0xffff0000, v34\n"
      "v_pk_mul_f32 v[56:57], v[56:57], s[34:35]\n"
      "v_cvt_pk_bf16_f32 v34, v56, v57\n"
      "v_lshlrev_b32 v58, 16, v35\n"
      "v_and_b32 v59, 0xffff0000, v35\n"
      "v_pk_mul_f32 v[58:59], v[58:59], s[34:35]\n"
      "v_cvt_pk_bf16_f32 v35, v58, v59\n"
      "ds_write_b128 v31, v[32:35] offset:0\n"
      "v_lshlrev_b32 v56, 16, v36\n"
      "v_and_b32 v57, 0xffff0000, v36\n"
      "v_pk_mul_f32 v[56:57], v[56:57], s[34:35]\n"
      "v_cvt_pk_bf16_f32 v36, v56, v57\n"
      "v_lshlrev_b32 v58, 16, v37\n"
      "v_and_b32 v59, 0xffff0000, v37\n"
      "v_pk_mul_f32 v[58:59], v[58:59], s[34:35]\n"
      "v_cvt_pk_bf16_f32 v37, v58, v59\n"
      "v_lshlrev_b32 v56, 16, v38\n"
      "v_and_b32 v57, 0xffff0000, v38\n"
      "v_pk_mul_f32 v[56:57], v[56:57], s[34:35]\n"
      "v_cvt_pk_bf16_f32 v38, v56, v57\n"
      "v_lshlrev_b32 v58, 16, v39\n"
      "v_and_b32 v59, 0xffff0000, v39\n"
      "v_pk_mul_f32 v[58:59], v[58:59], s[34:35]\n"
      "v_cvt_pk_bf16_f32 v39, v58, v59\n"
      "ds_write_b128 v31, v[36:39] offset:1024\n"
      "v_lshlrev_b32 v56, 16, v40\n"
      "v_and_b32 v57, 0xffff0000, v40\n"
      "v_pk_mul_f32 v[56:57], v[56:57], s[34:35]\n"
      "v_cvt_pk_bf16_f32 v40, v56, v57\n"
      "v_lshlrev_b32 v58, 16, v41\n"
      "v_and_b32 v59, 0xffff0000, v41\n"
      "v_pk_mul_f32 v[58:59], v[58:59], s[34:35]\n"
      "v_cvt_pk_bf16_f32 v41, v58, v59\n"
      "v_lshlrev_b32 v56, 16, v42\n"
      "v_and_b32 v57, 0xffff0000, v42\n"
      "v_pk_mul_f32 v[56:57], v[56:57], s[34:35]\n"
      "v_cvt_pk_bf16_f32 v42, v56, v57\n"
      "v_lshlrev_b32 v58, 16, v43\n"
      "v_and_b32 v59, 0xffff0000, v43\n"
      "v_pk_mul_f32 v[58:59], v[58:59], s[34:35]\n"
      "v_cvt_pk_bf16_f32 v43, v58, v59\n"
      "ds_write_b128 v31, v[40:43] offset:2048\n"
      "v_lshlrev_b32 v56, 16, v44\n"
      "v_and_b32 v57, 0xffff0000, v44\n"
      "v_pk_mul_f32 v[56:57], v[56:57], s[34:35]\n"
      "v_cvt_pk_bf16_f32 v44, v56, v57\n"
      "v_lshlrev_b32 v58, 16, v45\n"
      "v_and_b32 v59, 0xffff0000, v45\n"
      "v_pk_mul_f32 v[58:59], v[58:59], s[34:35]\n"
      "v_cvt_pk_bf16_f32 v45, v58, v59\n"
      "v_lshlrev_b32 v56, 16, v46\n"
      "v_and_b32 v57, 0xffff0000, v46\n"
      "v_pk_mul_f32 v[56:57], v[56:57], s[34:35]\n"
      "v_cvt_pk_bf16_f32 v46, v56, v57\n"
      "v_lshlrev_b32 v58, 16, v47\n"
      "v_and_b32 v59, 0xffff0000, v47\n"
      "v_pk_mul_f32 v[58:59], v[58:59], s[34:35]\n"
      "v_cvt_pk_bf16_f32 v47, v58, v59\n"
      "ds_write_b128 v31, v[44:47] offset:3072\n"
      "v_lshlrev_b32 v56, 16, v48\n"
      "v_and_b32 v57, 0xffff0000, v48\n"
      "v_pk_mul_f32 v[56:57], v[56:57], s[34:35]\n"
      "v_cvt_pk_bf16_f32 v48, v56, v57\n"
      "v_lshlrev_b32 v58, 16, v49\n"
      "v_and_b32 v59, 0xffff0000, v49\n"
      "v_pk_mul_f32 v[58:59], v[58:59], s[34:35]\n"
      "v_cvt_pk_bf16_f32 v49, v58, v59\n"
      "v_lshlrev_b32 v56, 16, v50\n"
      "v_and_b32 v57, 0xffff0000, v50\n"
      "v_pk_mul_f32 v[56:57], v[56:57], s[34:35]\n"
      "v_cvt_pk_bf16_f32 v50, v56, v57\n"
      "v_lshlrev_b32 v58, 16, v51\n"
      "v_and_b32 v59, 0xffff0000, v51\n"
      "v_pk_mul_f32 v[58:59], v[58:59], s[34:35]\n"
      "v_cvt_pk_bf16_f32 v51, v58, v59\n"
      "ds_write_b128 v31, v[48:51] offset:4096\n"
      "v_lshlrev_b32 v56, 16, v52\n"
      "v_and_b32 v57, 0xffff0000, v52\n"
      "v_pk_mul_f32 v[56:57], v[56:57], s[34:35]\n"
      "v_cvt_pk_bf16_f32 v52, v56, v57\n"
      "v_lshlrev_b32 v58, 16, v53\n"
      "v_and_b32 v59, 0xffff0000, v53\n"
      "v_pk_mul_f32 v[58:59], v[58:59], s[34:35]\n"
      "v_cvt_pk_bf16_f32 v53, v58, v59\n"
      "v_lshlrev_b32 v56, 16, v54\n"
      "v_and_b32 v57, 0xffff0000, v54\n"
      "v_pk_mul_f32 v[56:57], v[56:57], s[34:35]\n"
      "v_cvt_pk_bf16_f32 v54, v56, v57\n"
      "v_lshlrev_b32 v58, 16, v55\n"
      "v_and_b32 v59, 0xffff0000, v55\n"
      "v_pk_mul_f32 v[58:59], v[58:59], s[34:35]\n"
      "v_cvt_pk_bf16_f32 v55, v58, v59\n"
      "ds_write_b128 v31, v[52:55] offset:5120\n"
      "s_waitcnt lgkmcnt(0)\n"
      "s_barrier\n"
      "s_cmp_eq_u32 s37, 0\n"
      "s_cbranch_scc1 FQ0_%=\n"
      "ds_read_b128 v[160:163], v64 offset:0\n"
      "ds_read_b128 v[164:167], v66 offset:0\n"
      "ds_read_b128 v[168:171], v68 offset:0\n"
      "ds_read_b128 v[172:175], v70 offset:0\n"
      "ds_read_b128 v[176:179], v65 offset:0\n"
      "ds_read_b128 v[180:183], v67 offset:0\n"
      "ds_read_b128 v[184:187], v69 offset:0\n"
      "ds_read_b128 v[188:191], v71 offset:0\n"
      "ds_read_b128 v[192:195], v64 offset:2048\n"
      "ds_read_b128 v[196:199], v66 offset:2048\n"
      "ds_read_b128 v[200:203], v68 offset:2048\n"
      "ds_read_b128 v[204:207], v70 offset:2048\n"
      "ds_read_b128 v[208:211], v65 offset:2048\n"
      "ds_read_b128 v[212:215], v67 offset:2048\n"
      "ds_read_b128 v[216:219], v69 offset:2048\n"
      "ds_read_b128 v[220:223], v71 offset:2048\n"
      "ds_read_b128 v[224:227], v64 offset:4096\n"
      "ds_read_b128 v[228:231], v66 offset:4096\n"
      "ds_read_b128 v[232:235], v68 offset:4096\n"
      "ds_read_b128 v[236:239], v70 offset:4096\n"
      "ds_read_b128 v[240:243], v65 offset:4096\n"
      "ds_read_b128 v[244:247], v67 offset:4096\n"
      "ds_read_b128 v[248:251], v69 offset:4096\n"
      "ds_read_b128 v[252:255], v71 offset:4096\n"
      "s_branch FQD_%=\n"
      "FQ0_%=:\n"
      "ds_read_b128 v[160:163], v65 offset:2048\n"
      "ds_read_b128 v[164:167], v67 offset:2048\n"
      "ds_read_b128 v[168:171], v69 offset:2048\n"
      "ds_read_b128 v[172:175], v71 offset:2048\n"
      "ds_read_b128 v[176:179], v64 offset:4096\n"
      "ds_read_b128 v[180:183], v66 offset:4096\n"
      "ds_read_b128 v[184:187], v68 offset:4096\n"
      "ds_read_b128 v[188:191], v70 offset:4096\n"
      "ds_read_b128 v[192:195], v65 offset:4096\n"
      "ds_read_b128 v[196:199], v67 offset:4096\n"
      "ds_read_b128 v[200:203], v69 offset:4096\n"
      "ds_read_b128 v[204:207], v71 offset:4096\n"
      "ds_read_b128 v[208:211], v64 offset:0\n"
      "ds_read_b128 v[212:215], v66 offset:0\n"
      "ds_read_b128 v[216:219], v68 offset:0\n"
      "ds_read_b128 v[220:223], v70 offset:0\n"
      "ds_read_b128 v[224:227], v65 offset:0\n"
      "ds_read_b128 v[228:231], v67 offset:0\n"
      "ds_read_b128 v[232:235], v69 offset:0\n"
      "ds_read_b128 v[236:239], v71 offset:0\n"
      "ds_read_b128 v[240:243], v64 offset:2048\n"
      "ds_read_b128 v[244:247], v66 offset:2048\n"
      "ds_read_b128 v[248:251], v68 offset:2048\n"
      "ds_read_b128 v[252:255], v70 offset:2048\n"
      "FQD_%=:\n"
      "FBL_%=:\n"
      "s_xor_b32 s64, s63, 0x4000\n"
      "s_mov_b32 m0, s40\n"
      "s_nop 0\n"
      "buffer_load_dwordx4 v1, s[20:23], s63 offen offset:0 lds\n"
      "buffer_load_dwordx4 v2, s[20:23], s63 offen offset:1024 lds\n"
      "buffer_load_dwordx4 v3, s[20:23], s63 offen offset:2048 lds\n"
      "buffer_load_dwordx4 v4, s[20:23], s63 offen offset:3072 lds\n"
      "s_mov_b32 m0, s52\n"
      "s_nop 0\n"
      "buffer_load_dwordx4 v1, s[20:23], s64 offen offset:0 lds\n"
      "buffer_load_dwordx4 v2, s[20:23], s64 offen offset:1024 lds\n"
      "buffer_load_dwordx4 v3, s[20:23], s64 offen offset:2048 lds\n"
      "buffer_load_dwordx4 v4, s[20:23], s64 offen offset:3072 lds\n"
      "s_waitcnt vmcnt(0)\n"
      "ds_read_b128 a[192:195], v5 offset:0\n"
      "ds_read_b128 a[196:199], v6 offset:0\n"
      "ds_read_b128 a[200:203], v5 offset:512\n"
      "ds_read_b128 a[204:207], v6 offset:512\n"
      "ds_read_b128 a[208:211], v5 offset:1024\n"
      "ds_read_b128 a[212:215], v6 offset:1024\n"
      "ds_read_b128 a[216:219], v5 offset:1536\n"
      "ds_read_b128 a[220:223], v6 offset:1536\n"
      "s_waitcnt lgkmcnt(0)\n"
      "v_mfma_f32_16x16x32_bf16 v[32:35], a[192:195], v[160:163], 0\n"
      "v_mfma_f32_16x16x32_bf16 v[40:43], a[192:195], v[176:179], 0\n"
      "v_mfma_f32_16x16x32_bf16 v[36:39], a[196:199], v[160:163], 0\n"
      "v_mfma_f32_16x16x32_bf16 v[44:47], a[196:199], v[176:179], 0\n"
      "v_mfma_f32_16x16x32_bf16 v[32:35], a[200:203], v[164:167], v[32:35]\n"
      "v_mfma_f32_16x16x32_bf16 v[40:43], a[200:203], v[180:183], v[40:43]\n"
      "v_mfma_f32_16x16x32_bf16 v[36:39], a[204:207], v[164:167], v[36:39]\n"
      "v_mfma_f32_16x16x32_bf16 v[44:47], a[204:207], v[180:183], v[44:47]\n"
      "v_mfma_f32_16x16x32_bf16 v[32:35], a[208:211], v[168:171], v[32:35]\n"
      "v_mfma_f32_16x16x32_bf16 v[40:43], a[208:211], v[184:187], v[40:43]\n"
      "v_mfma_f32_16x16x32_bf16 v[36:39], a[212:215], v[168:171], v[36:39]\n"
      "v_mfma_f32_16x16x32_bf16 v[44:47], a[212:215], v[184:187], v[44:47]\n"
      "v_mfma_f32_16x16x32_bf16 v[32:35], a[216:219], v[172:175], v[32:35]\n"
      "v_mfma_f32_16x16x32_bf16 v[40:43], a[216:219], v[188:191], v[40:43]\n"
      "v_mfma_f32_16x16x32_bf16 v[36:39], a[220:223], v[172:175], v[36:39]\n"
      "v_mfma_f32_16x16x32_bf16 v[44:47], a[220:223], v[188:191], v[44:47]\n"
      "v_mfma_f32_16x16x32_bf16 v[48:51], a[192:195], v[192:195], 0\n"
      "v_mfma_f32_16x16x32_bf16 v[56:59], a[192:195], v[208:211], 0\n"
      "v_mfma_f32_16x16x32_bf16 v[52:55], a[196:199], v[192:195], 0\n"
      "v_mfma_f32_16x16x32_bf16 v[60:63], a[196:199], v[208:211], 0\n"
      "v_mfma_f32_16x16x32_bf16 v[48:51], a[200:203], v[196:199], v[48:51]\n"
      "v_mfma_f32_16x16x32_bf16 v[56:59], a[200:203], v[212:215], v[56:59]\n"
      "v_mfma_f32_16x16x32_bf16 v[52:55], a[204:207], v[196:199], v[52:55]\n"
      "v_mfma_f32_16x16x32_bf16 v[60:63], a[204:207], v[212:215], v[60:63]\n"
      "v_mfma_f32_16x16x32_bf16 v[48:51], a[208:211], v[200:203], v[48:51]\n"
      "v_mfma_f32_16x16x32_bf16 v[56:59], a[208:211], v[216:219], v[56:59]\n"
      "v_mfma_f32_16x16x32_bf16 v[52:55], a[212:215], v[200:203], v[52:55]\n"
      "v_mfma_f32_16x16x32_bf16 v[60:63], a[212:215], v[216:219], v[60:63]\n"
      "v_mfma_f32_16x16x32_bf16 v[48:51], a[216:219], v[204:207], v[48:51]\n"
      "v_mfma_f32_16x16x32_bf16 v[56:59], a[216:219], v[220:223], v[56:59]\n"
      "v_mfma_f32_16x16x32_bf16 v[52:55], a[220:223], v[204:207], v[52:55]\n"
      "v_mfma_f32_16x16x32_bf16 v[60:63], a[220:223], v[220:223], v[60:63]\n"
      "v_mfma_f32_16x16x32_bf16 v[64:67], a[192:195], v[224:227], 0\n"
      "v_mfma_f32_16x16x32_bf16 v[72:75], a[192:195], v[240:243], 0\n"
      "v_mfma_f32_16x16x32_bf16 v[68:71], a[196:199], v[224:227], 0\n"
      "v_mfma_f32_16x16x32_bf16 v[76:79], a[196:199], v[240:243], 0\n"
      "v_mfma_f32_16x16x32_bf16 v[64:67], a[200:203], v[228:231], v[64:67]\n"
      "v_mfma_f32_16x16x32_bf16 v[72:75], a[200:203], v[244:247], v[72:75]\n"
      "v_mfma_f32_16x16x32_bf16 v[68:71], a[204:207], v[228:231], v[68:71]\n"
      "v_mfma_f32_16x16x32_bf16 v[76:79], a[204:207], v[244:247], v[76:79]\n"
      "v_mfma_f32_16x16x32_bf16 v[64:67], a[208:211], v[232:235], v[64:67]\n"
      "v_mfma_f32_16x16x32_bf16 v[72:75], a[208:211], v[248:251], v[72:75]\n"
      "v_mfma_f32_16x16x32_bf16 v[68:71], a[212:215], v[232:235], v[68:71]\n"
      "v_mfma_f32_16x16x32_bf16 v[76:79], a[212:215], v[248:251], v[76:79]\n"
      "v_mfma_f32_16x16x32_bf16 v[64:67], a[216:219], v[236:239], v[64:67]\n"
      "v_mfma_f32_16x16x32_bf16 v[72:75], a[216:219], v[252:255], v[72:75]\n"
      "v_mfma_f32_16x16x32_bf16 v[68:71], a[220:223], v[236:239], v[68:71]\n"
      "v_mfma_f32_16x16x32_bf16 v[76:79], a[220:223], v[252:255], v[76:79]\n"
      "v_max3_f32 v128, v128, v32, v33\n"
      "v_max3_f32 v128, v128, v34, v35\n"
      "v_max3_f32 v128, v128, v36, v37\n"
      "v_max3_f32 v128, v128, v38, v39\n"
      "v_max3_f32 v129, v129, v40, v41\n"
      "v_max3_f32 v129, v129, v42, v43\n"
      "v_max3_f32 v129, v129, v44, v45\n"
      "v_max3_f32 v129, v129, v46, v47\n"
      "v_max3_f32 v130, v130, v48, v49\n"
      "v_max3_f32 v130, v130, v50, v51\n"
      "v_max3_f32 v130, v130, v52, v53\n"
      "v_max3_f32 v130, v130, v54, v55\n"
      "v_max3_f32 v131, v131, v56, v57\n"
      "v_max3_f32 v131, v131, v58, v59\n"
      "v_max3_f32 v131, v131, v60, v61\n"
      "v_max3_f32 v131, v131, v62, v63\n"
      "v_max3_f32 v132, v132, v64, v65\n"
      "v_max3_f32 v132, v132, v66, v67\n"
      "v_max3_f32 v132, v132, v68, v69\n"
      "v_max3_f32 v132, v132, v70, v71\n"
      "v_max3_f32 v133, v133, v72, v73\n"
      "v_max3_f32 v133, v133, v74, v75\n"
      "v_max3_f32 v133, v133, v76, v77\n"
      "v_max3_f32 v133, v133, v78, v79\n"
      "s_add_u32 s63, s63, 0x8000\n"
      "s_sub_u32 s53, s53, 1\n"
      "s_cbranch_scc0 FBL_%=\n"
      "v_mov_b32 v29, v128\n"
      "s_nop 1\n"
      "v_permlane16_swap_b32 v128, v29\n"
      "v_max_f32 v128, v128, v29\n"
      "v_mov_b32 v29, v128\n"
      "s_nop 1\n"
      "v_permlane32_swap_b32 v128, v29\n"
      "v_max_f32 v128, v128, v29\n"
      "v_mov_b32 v29, v129\n"
      "s_nop 1\n"
      "v_permlane16_swap_b32 v129, v29\n"
      "v_max_f32 v129, v129, v29\n"
      "v_mov_b32 v29, v129\n"
      "s_nop 1\n"
      "v_permlane32_swap_b32 v129, v29\n"
      "v_max_f32 v129, v129, v29\n"
      "v_mov_b32 v29, v130\n"
      "s_nop 1\n"
      "v_permlane16_swap_b32 v130, v29\n"
      "v_max_f32 v130, v130, v29\n"
      "v_mov_b32 v29, v130\n"
      "s_nop 1\n"
      "v_permlane32_swap_b32 v130, v29\n"
      "v_max_f32 v130, v130, v29\n"
      "v_mov_b32 v29, v131\n"
      "s_nop 1\n"
      "v_permlane16_swap_b32 v131, v29\n"
      "v_max_f32 v131, v131, v29\n"
      "v_mov_b32 v29, v131\n"
      "s_nop 1\n"
      "v_permlane32_swap_b32 v131, v29\n"
      "v_max_f32 v131, v131, v29\n"
      "v_mov_b32 v29, v132\n"
      "s_nop 1\n"
      "v_permlane16_swap_b32 v132, v29\n"
      "v_max_f32 v132, v132, v29\n"
      "v_mov_b32 v29, v132\n"
      "s_nop 1\n"
      "v_permlane32_swap_b32 v132, v29\n"
      "v_max_f32 v132, v132, v29\n"
      "v_mov_b32 v29, v133\n"
      "s_nop 1\n"
      "v_permlane16_swap_b32 v133, v29\n"
      "v_max_f32 v133, v133, v29\n"
      "v_mov_b32 v29, v133\n"
      "s_nop 1\n"
      "v_permlane32_swap_b32 v133, v29\n"
      "v_max_f32 v133, v133, v29\n"
      "v_cndmask_b32 v12, v128, v129, s[56:57]\n"
      "v_cndmask_b32 v12, v12, v130, s[58:59]\n"
      "v_cndmask_b32 v13, v131, v132, s[56:57]\n"
      "v_cndmask_b32 v13, v13, v133, s[58:59]\n"
      "ds_write_b64 v158, v[12:13]\n"
      "s_waitcnt lgkmcnt(0)\n"
      "s_barrier\n"
      "v_xor_b32 v26, 0x2000, v158\n"
      "ds_read_b64 v[136:137], v26\n"
      "v_xor_b32 v26, 0x4000, v158\n"
      "ds_read_b64 v[138:139], v26\n"
      "v_xor_b32 v26, 0x6000, v158\n"
      "ds_read_b64 v[140:141], v26\n"
      "s_mov_b32 s63, s65\n"
      "s_xor_b32 s64, s63, 0x4000\n"
      "s_mov_b32 m0, s40\n"
      "s_nop 0\n"
      "buffer_load_dwordx4 v1, s[20:23], s63 offen offset:0 nt lds\n"
      "buffer_load_dwordx4 v2, s[20:23], s63 offen offset:1024 nt lds\n"
      "buffer_load_dwordx4 v3, s[20:23], s63 offen offset:2048 nt lds\n"
      "buffer_load_dwordx4 v4, s[20:23], s63 offen offset:3072 nt lds\n"
      "s_add_u32 m0, s40, 0x1000\n"
      "s_nop 0\n"
      "buffer_load_dwordx4 v1, s[20:23], s64 offen offset:0 nt lds\n"
      "buffer_load_dwordx4 v2, s[20:23], s64 offen offset:1024 nt lds\n"
      "buffer_load_dwordx4 v3, s[20:23], s64 offen offset:2048 nt lds\n"
      "buffer_load_dwordx4 v4, s[20:23], s64 offen offset:3072 nt lds\n"
      "s_add_u32 m0, s40, 0x2000\n"
      "s_nop 0\n"
      "buffer_load_dwordx4 v1, s[24:27], s63 offen offset:0 nt lds\n"
      "buffer_load_dwordx4 v2, s[24:27], s63 offen offset:1024 nt lds\n"
      "buffer_load_dwordx4 v3, s[24:27], s63 offen offset:2048 nt lds\n"
      "buffer_load_dwordx4 v4, s[24:27], s63 offen offset:3072 nt lds\n"
      "s_waitcnt lgkmcnt(0)\n"
      "v_max3_f32 v12, v12, v136, v138\n"
      "v_max_f32 v12, v12, v140\n"
      "v_max3_f32 v13, v13, v137, v139\n"
      "v_max_f32 v13, v13, v141\n"
      "v_sub_f32 v104, 0, v12\n"
      "v_mov_b32 v108, v104\n"
      "s_nop 1\n"
      "v_permlane16_swap_b32 v104, v108\n"
      "v_mov_b32 v112, v104\n"
      "v_mov_b32 v29, v108\n"
      "s_nop 0\n"
      "v_permlane32_swap_b32 v104, v112\n"
      "v_permlane32_swap_b32 v108, v29\n"
      "v_sub_f32 v116, 0, v13\n"
      "v_mov_b32 v120, v116\n"
      "s_nop 1\n"
      "v_permlane16_swap_b32 v116, v120\n"
      "v_mov_b32 v124, v116\n"
      "v_mov_b32 v29, v120\n"
      "s_nop 0\n"
      "v_permlane32_swap_b32 v116, v124\n"
      "v_permlane32_swap_b32 v120, v29\n"
      "v_mov_b32 v105, v104\n"
      "v_mov_b32 v106, v104\n"
      "v_mov_b32 v107, v104\n"
      "v_mov_b32 v109, v108\n"
      "v_mov_b32 v110, v108\n"
      "v_mov_b32 v111, v108\n"
      "v_mov_b32 v113, v112\n"
      "v_mov_b32 v114, v112\n"
      "v_mov_b32 v115, v112\n"
      "v_mov_b32 v117, v116\n"
      "v_mov_b32 v118, v116\n"
      "v_mov_b32 v119, v116\n"
      "v_mov_b32 v121, v120\n"
      "v_mov_b32 v122, v120\n"
      "v_mov_b32 v123, v120\n"
      "v_mov_b32 v125, v124\n"
      "v_mov_b32 v126, v124\n"
      "v_mov_b32 v127, v124\n"
      "s_waitcnt vmcnt(4)\n"
      "ds_read_b128 a[192:195], v5 offset:0\n"
      "ds_read_b128 a[196:199], v6 offset:0\n"
      "ds_read_b128 a[200:203], v5 offset:512\n"
      "ds_read_b128 a[204:207], v6 offset:512\n"
      "ds_read_b128 a[208:211], v5 offset:1024\n"
      "ds_read_b128 a[212:215], v6 offset:1024\n"
      "ds_read_b128 a[216:219], v5 offset:1536\n"
      "ds_read_b128 a[220:223], v6 offset:1536\n"
      "s_waitcnt lgkmcnt(0)\n"
      "v_mfma_f32_16x16x32_bf16 v[32:35], a[192:195], v[160:163], v[104:107]\n"
      "v_mfma_f32_16x16x32_bf16 v[40:43], a[192:195], v[176:179], v[108:111]\n"
      "v_mfma_f32_16x16x32_bf16 v[36:39], a[196:199], v[160:163], v[104:107]\n"
      "v_mfma_f32_16x16x32_bf16 v[44:47], a[196:199], v[176:179], v[108:111]\n"
      "v_mfma_f32_16x16x32_bf16 v[32:35], a[200:203], v[164:167], v[32:35]\n"
      "v_mfma_f32_16x16x32_bf16 v[40:43], a[200:203], v[180:183], v[40:43]\n"
      "v_mfma_f32_16x16x32_bf16 v[36:39], a[204:207], v[164:167], v[36:39]\n"
      "v_mfma_f32_16x16x32_bf16 v[44:47], a[204:207], v[180:183], v[44:47]\n"
      "v_mfma_f32_16x16x32_bf16 v[32:35], a[208:211], v[168:171], v[32:35]\n"
      "v_mfma_f32_16x16x32_bf16 v[40:43], a[208:211], v[184:187], v[40:43]\n"
      "v_mfma_f32_16x16x32_bf16 v[36:39], a[212:215], v[168:171], v[36:39]\n"
      "v_mfma_f32_16x16x32_bf16 v[44:47], a[212:215], v[184:187], v[44:47]\n"
      "v_mfma_f32_16x16x32_bf16 v[32:35], a[216:219], v[172:175], v[32:35]\n"
      "v_mfma_f32_16x16x32_bf16 v[40:43], a[216:219], v[188:191], v[40:43]\n"
      "v_mfma_f32_16x16x32_bf16 v[36:39], a[220:223], v[172:175], v[36:39]\n"
      "v_mfma_f32_16x16x32_bf16 v[44:47], a[220:223], v[188:191], v[44:47]\n"
      "s_add_u32 m0, s40, 0x3000\n"
      "s_nop 0\n"
      "buffer_load_dwordx4 v1, s[24:27], s64 offen offset:0 nt lds\n"
      "buffer_load_dwordx4 v2, s[24:27], s64 offen offset:1024 nt lds\n"
      "buffer_load_dwordx4 v3, s[24:27], s64 offen offset:2048 nt lds\n"
      "buffer_load_dwordx4 v4, s[24:27], s64 offen offset:3072 nt lds\n"
      "s_add_u32 m0, s40, 0x4000\n"
      "s_add_u32 s63, s63, 0x8000\n"
      "buffer_load_dwordx4 v1, s[20:23], s63 offen offset:0 nt lds\n"
      "buffer_load_dwordx4 v2, s[20:23], s63 offen offset:1024 nt lds\n"
      "buffer_load_dwordx4 v3, s[20:23], s63 offen offset:2048 nt lds\n"
      "buffer_load_dwordx4 v4, s[20:23], s63 offen offset:3072 nt lds\n"
      "s_add_u32 m0, s40, 0x5000\n"
      "s_add_u32 s64, s64, 0x8000\n"
      "buffer_load_dwordx4 v1, s[20:23], s64 offen offset:0 nt lds\n"
      "buffer_load_dwordx4 v2, s[20:23], s64 offen offset:1024 nt lds\n"
      "buffer_load_dwordx4 v3, s[20:23], s64 offen offset:2048 nt lds\n"
      "buffer_load_dwordx4 v4, s[20:23], s64 offen offset:3072 nt lds\n"
      "s_add_u32 m0, s40, 0x6000\n"
      "s_nop 0\n"
      "buffer_load_dwordx4 v1, s[24:27], s63 offen offset:0 nt lds\n"
      "buffer_load_dwordx4 v2, s[24:27], s63 offen offset:1024 nt lds\n"
      "buffer_load_dwordx4 v3, s[24:27], s63 offen offset:2048 nt lds\n"
      "buffer_load_dwordx4 v4, s[24:27], s63 offen offset:3072 nt lds\n"
      "s_add_u32 m0, s40, 0x7000\n"
      "s_nop 0\n"
      "buffer_load_dwordx4 v1, s[24:27], s64 offen offset:0 nt lds\n"
      "buffer_load_dwordx4 v2, s[24:27], s64 offen offset:1024 nt lds\n"
      "buffer_load_dwordx4 v3, s[24:27], s64 offen offset:2048 nt lds\n"
      "buffer_load_dwordx4 v4, s[24:27], s64 offen offset:3072 nt lds\n"
      "s_nop 7\n"
      "s_branch ENTRY_%=\n"
      :
      : "s"(__builtin_amdgcn_kernarg_segment_ptr()), "s"((unsigned)blockIdx.x), "s"(wave)
      : "v0", "v1", "v2", "v3", "v4", "v5", "v6", "v7", "v8", "v9", "v10", "v11", "v12", "v13", "v14", "v15", "v16", "v17", "v18", "v19", "v20", "v21", "v22", "v23", "v24", "v25", "v26", "v27", "v28", "v29", "v30", "v31", "v32", "v33", "v34", "v35", "v36", "v37", "v38", "v39", "v40", "v41", "v42", "v43", "v44", "v45", "v46", "v47", "v48", "v49", "v50", "v51", "v52", "v53", "v54", "v55", "v56", "v57", "v58", "v59", "v60", "v61", "v62", "v63", "v64", "v65", "v66", "v67", "v68", "v69", "v70", "v71", "v72", "v73", "v74", "v75", "v76", "v77", "v78", "v79", "v80", "v81", "v82", "v83", "v84", "v85", "v86", "v87", "v88", "v89", "v90", "v91", "v92", "v93", "v94", "v95", "v96", "v97", "v98", "v99", "v100", "v101", "v102", "v103", "v104", "v105", "v106", "v107", "v108", "v109", "v110", "v111", "v112", "v113", "v114", "v115", "v116", "v117", "v118", "v119", "v120", "v121", "v122", "v123", "v124", "v125", "v126", "v127", "v128", "v129", "v130", "v131", "v132", "v133", "v134", "v135", "v136", "v137", "v138", "v139", "v140", "v141", "v142", "v143", "v144", "v145", "v146", "v147", "v148", "v149", "v150", "v151", "v152", "v153", "v154", "v155", "v156", "v157", "v158", "v159", "v160", "v161", "v162", "v163", "v164", "v165", "v166", "v167", "v168", "v169", "v170", "v171", "v172", "v173", "v174", "v175", "v176", "v177", "v178", "v179", "v180", "v181", "v182", "v183", "v184", "v185", "v186", "v187", "v188", "v189", "v190", "v191", "v192", "v193", "v194", "v195", "v196", "v197", "v198", "v199", "v200", "v201", "v202", "v203", "v204", "v205", "v206", "v207", "v208", "v209", "v210", "v211", "v212", "v213", "v214", "v215", "v216", "v217", "v218", "v219", "v220", "v221", "v222", "v223", "v224", "v225", "v226", "v227", "v228", "v229", "v230", "v231", "v232", "v233", "v234", "v235", "v236", "v237", "v238", "v239", "v240", "v241", "v242", "v243", "v244", "v245", "v246", "v247", "v248", "v249", "v250", "v251", "v252", "v253", "v254", "v255", "a0", "a1", "a2", "a3", "a4", "a5", "a6", "a7", "a8", "a9", "a10", "a11", "a12", "a13", "a14", "a15", "a16", "a17", "a18", "a19", "a20", "a21", "a22", "a23", "a24", "a25", "a26", "a27", "a28", "a29", "a30", "a31", "a32", "a33", "a34", "a35", "a36", "a37", "a38", "a39", "a40", "a41", "a42", "a43", "a44", "a45", "a46", "a47", "a48", "a49", "a50", "a51", "a52", "a53", "a54", "a55", "a56", "a57", "a58", "a59", "a60", "a61", "a62", "a63", "a64", "a65", "a66", "a67", "a68", "a69", "a70", "a71", "a72", "a73", "a74", "a75", "a76", "a77", "a78", "a79", "a80", "a81", "a82", "a83", "a84", "a85", "a86", "a87", "a88", "a89", "a90", "a91", "a92", "a93", "a94", "a95", "a96", "a97", "a98", "a99", "a100", "a101", "a102", "a103", "a104", "a105", "a106", "a107", "a108", "a109", "a110", "a111", "a112", "a113", "a114", "a115", "a116", "a117", "a118", "a119", "a120", "a121", "a122", "a123", "a124", "a125", "a126", "a127", "a128", "a129", "a130", "a131", "a132", "a133", "a134", "a135", "a136", "a137", "a138", "a139", "a140", "a141", "a142", "a143", "a144", "a145", "a146", "a147", "a148", "a149", "a150", "a151", "a152", "a153", "a154", "a155", "a156", "a157", "a158", "a159", "a160", "a161", "a162", "a163", "a164", "a165", "a166", "a167", "a168", "a169", "a170", "a171", "a172", "a173", "a174", "a175", "a176", "a177", "a178", "a179", "a180", "a181", "a182", "a183", "a184", "a185", "a186", "a187", "a188", "a189", "a190", "a191", "a192", "a193", "a194", "a195", "a196", "a197", "a198", "a199", "a200", "a201", "a202", "a203", "a204", "a205", "a206", "a207", "a208", "a209", "a210", "a211", "a212", "a213", "a214", "a215", "a216", "a217", "a218", "a219", "a220", "a221", "a222", "a223", "a224", "a225", "a226", "a227", "a228", "a229", "a230", "a231", "a232", "a233", "a234", "a235", "a236", "a237", "a238", "a239", "a240", "a241", "a242", "a243", "a244", "a245", "a246", "a247", "a248", "a249", "a250", "a251", "a252", "a253", "a254", "a255", "s4", "s5", "s6", "s7", "s8", "s9", "s10", "s11", "s12", "s13", "s16", "s17", "s18", "s19", "s20", "s21", "s22", "s23", "s24", "s25", "s26", "s27", "s28", "s29", "s30", "s31", "s34", "s35", "s36", "s37", "s38", "s39", "s40", "s41", "s42", "s43", "s44", "s45", "s46", "s47", "s48", "s49", "s50", "s51", "s52", "s53", "s54", "s55", "s56", "s57", "s58", "s59", "s60", "s61", "s62", "s63", "s64", "s65", "s66", "s67", "vcc", "scc", "memory");
}

extern "C" void kernel_launch(void* const* d_in, const int* in_sizes, int n_in, void* d_out, int out_size, void* d_ws, size_t ws_size,
                              hipStream_t stream) {
  (void)in_sizes; (void)n_in; (void)out_size; (void)ws_size;
  (void)hipFuncSetAttribute((const void*)gqa_attn_kernel, hipFuncAttributeMaxDynamicSharedMemorySize, 163840);
  gqa_attn_kernel<<<256, 256, 163840, stream>>>(d_in[0], d_in[1], d_in[2], d_out, d_ws);
}
```
